```python
import torch

def _softmax(x, axis=-1):
    return torch.softmax(x, dim=axis)
def _gelu(x):
    return torch.nn.functional.gelu(x, approximate="tanh")
def _silu(x):
    return torch.nn.functional.silu(x)
def _sigmoid(x):
    return torch.sigmoid(x)
def _layernorm(x, axis=-1, eps=1e-5):
    m = x.mean(dim=axis, keepdim=True)
    v = x.var(dim=axis, keepdim=True, unbiased=False)
    return (x - m) / torch.sqrt(v + eps)
def _scatter_add(tgt, idx, src, axis=0):
    return tgt.clone().index_add_(axis, idx, src)
def _segment_sum(src, seg, n):
    out = torch.zeros((n,) + src.shape[1:], dtype=src.dtype)
    return out.index_add_(0, seg, src)
def _pad(x, pw):
    flat = tuple(v for pair in reversed(pw) for v in pair)
    return torch.nn.functional.pad(x, flat)

kernel_name = 'concat_heads__gqa_block_cached__mha_block_cached__near_ridge'

def setup_inputs():

    _w = (lambda a: (lambda t: t.to(torch.bfloat16) if t.is_floating_point() else t)(torch.as_tensor(a)))
    def _A_setup():
        B, Sq, D = 16, 32, 1024
        Hq, Hkv, Sk, Dh = 32, 4, 512, 128
        assert Hq % Hkv == 0, (Hq, Hkv)
        return {
            "x": _w((lambda *s: torch.randn(*s, dtype=torch.bfloat16) * 0.8)(B, Sq, D)),
            "k_cache": _w((lambda *s: torch.randn(*s, dtype=torch.bfloat16) * 0.8)(B, Hkv, Sk, Dh)),
            "v_cache": _w((lambda *s: torch.randn(*s, dtype=torch.bfloat16) * 0.05)(B, Hkv, Sk, Dh)),
            "wq": _w((lambda *s: torch.randn(*s, dtype=torch.bfloat16) * 0.8)(D, Hq * Dh) * (D ** -0.5)),
            "wo": _w((lambda *s: torch.randn(*s, dtype=torch.bfloat16) * 0.05)(Hq * Dh, D) * ((Hq * Dh) ** -0.5)),
        }
    _A_in = _A_setup()

    def _B_setup():
        B, Sq, D = 16, 32, 1024
        H, Sk, Dh = 32, 512, 128
        return {
            "x": _w((lambda *s: torch.randn(*s, dtype=torch.bfloat16) * 0.8)(B, Sq, D)),
            "k_cache": _w((lambda *s: torch.randn(*s, dtype=torch.bfloat16) * 0.8)(B, H, Sk, Dh)),
            "v_cache": _w((lambda *s: torch.randn(*s, dtype=torch.bfloat16) * 0.05)(B, H, Sk, Dh)),
            "wq": _w((lambda *s: torch.randn(*s, dtype=torch.bfloat16) * 0.8)(D, H * Dh) * (D ** -0.5)),
            "wo": _w((lambda *s: torch.randn(*s, dtype=torch.bfloat16) * 0.05)(H * Dh, D) * ((H * Dh) ** -0.5)),
        }
    _B_in = _B_setup()

    return {
        "A_x": _A_in["x"],
        "A_k_cache": _A_in["k_cache"],
        "A_v_cache": _A_in["v_cache"],
        "A_wq": _A_in["wq"],
        "A_wo": _A_in["wo"],
        "B_x": _B_in["x"],
        "B_k_cache": _B_in["k_cache"],
        "B_v_cache": _B_in["v_cache"],
        "B_wq": _B_in["wq"],
        "B_wo": _B_in["wo"],
    }

def reference(A_x, A_k_cache, A_v_cache, A_wq, A_wo, B_x, B_k_cache, B_v_cache, B_wq, B_wo):
    def _A_ref(x, k_cache, v_cache, wq, wo):
        B, Sq, D = x.shape
        Hq, Dh = 32, 128
        Hkv = k_cache.shape[1]
        rep = Hq // Hkv
        q = (lambda x, *d: x.permute(*d))((x @ wq).reshape(B, Sq, Hq, Dh), 0, 2, 1, 3)
        k = (lambda x, n, axis: torch.repeat_interleave(x, n, dim=axis))(k_cache, rep, axis=1)
        v = (lambda x, n, axis: torch.repeat_interleave(x, n, dim=axis))(v_cache, rep, axis=1)
        scores = torch.einsum("bhqd,bhkd->bhqk", q, k) * (Dh ** -0.5)
        p = _softmax(scores, axis=-1)
        o = torch.einsum("bhqk,bhkd->bhqd", p, v)
        return (lambda x, *d: x.permute(*d))(o, 0, 2, 1, 3).reshape(B, Sq, Hq * Dh) @ wo

    def _B_ref(x, k_cache, v_cache, wq, wo):
        B, Sq, D = x.shape
        H, Dh = 32, 128
        q = (lambda x, *d: x.permute(*d))((x @ wq).reshape(B, Sq, H, Dh), 0, 2, 1, 3)
        scores = torch.einsum("bhqd,bhkd->bhqk", q, k_cache) * (Dh ** -0.5)
        p = _softmax(scores, axis=-1)
        o = torch.einsum("bhqk,bhkd->bhqd", p, v_cache)
        return (lambda x, *d: x.permute(*d))(o, 0, 2, 1, 3).reshape(B, Sq, H * Dh) @ wo

    a = _A_ref(A_x, A_k_cache, A_v_cache, A_wq, A_wo)
    b = _B_ref(B_x, B_k_cache, B_v_cache, B_wq, B_wo)
    return torch.concatenate([a, b], axis=-1)
```

```cpp
#include <hip/hip_runtime.h>
#include <hip/hip_bf16.h>

typedef short bf16x8 __attribute__((ext_vector_type(8)));
typedef short s16x4 __attribute__((ext_vector_type(4)));
typedef float f32x4 __attribute__((ext_vector_type(4)));
typedef float f32x2 __attribute__((ext_vector_type(2)));
typedef __bf16 bf16x2 __attribute__((ext_vector_type(2)));
typedef unsigned u32x4 __attribute__((ext_vector_type(4)));
typedef unsigned u32x2 __attribute__((ext_vector_type(2)));
#define LAS __attribute__((address_space(3)))
#define DI __device__ __forceinline__

#define WAIT_V(n) asm volatile("s_waitcnt vmcnt(%0)" ::"n"(n) : "memory")
#define WAIT_L0() asm volatile("s_waitcnt lgkmcnt(0)" ::: "memory")
#define BAR() __builtin_amdgcn_s_barrier()
#define SB() __builtin_amdgcn_sched_barrier(0)

template <int IMM, bool NT>
DI void glds(const char* sbase, unsigned voff, unsigned ldsaddr) {
  if (NT)
    asm volatile("s_mov_b32 m0, %2\n\ts_nop 0\n\tglobal_load_lds_dwordx4 %0, %1 offset:%3 nt" ::"v"(voff), "s"(sbase), "s"(ldsaddr), "i"(IMM) : "memory");
  else
    asm volatile("s_mov_b32 m0, %2\n\ts_nop 0\n\tglobal_load_lds_dwordx4 %0, %1 offset:%3" ::"v"(voff), "s"(sbase), "s"(ldsaddr), "i"(IMM) : "memory");
}
template <bool NT>
DI void glds4(const char* sbase, unsigned v0, unsigned v1, unsigned v2, unsigned v3, unsigned ldsaddr) {
  if (NT)
    asm volatile("s_mov_b32 m0, %5\n\ts_nop 0\n\t"
                 "global_load_lds_dwordx4 %0, %4 nt\n\t"
                 "global_load_lds_dwordx4 %1, %4 offset:1024 nt\n\t"
                 "global_load_lds_dwordx4 %2, %4 offset:2048 nt\n\t"
                 "global_load_lds_dwordx4 %3, %4 offset:3072 nt"
                 ::"v"(v0), "v"(v1), "v"(v2), "v"(v3), "s"(sbase), "s"(ldsaddr) : "memory");
  else
    asm volatile("s_mov_b32 m0, %5\n\ts_nop 0\n\t"
                 "global_load_lds_dwordx4 %0, %4\n\t"
                 "global_load_lds_dwordx4 %1, %4 offset:1024\n\t"
                 "global_load_lds_dwordx4 %2, %4 offset:2048\n\t"
                 "global_load_lds_dwordx4 %3, %4 offset:3072"
                 ::"v"(v0), "v"(v1), "v"(v2), "v"(v3), "s"(sbase), "s"(ldsaddr) : "memory");
}

DI f32x4 mfma16(bf16x8 a, bf16x8 b, f32x4 c) { return __builtin_amdgcn_mfma_f32_16x16x32_bf16(a, b, c, 0, 0, 0); }
DI unsigned pk2(float lo, float hi) { f32x2 a = {lo, hi}; return __builtin_bit_cast(unsigned, __builtin_convertvector(a, bf16x2)); }
DI bf16x8 lds_b128(const char* smem, unsigned off) { return *(const bf16x8*)(smem + off); }
DI s16x4 lds_tr(char* smem, unsigned off) { return __builtin_amdgcn_ds_read_tr16_b64_v4i16((LAS s16x4*)(smem + off)); }
DI bf16x8 cat8(s16x4 lo, s16x4 hi) { return __builtin_shufflevector(lo, hi, 0, 1, 2, 3, 4, 5, 6, 7); }
DI float xg_max(float x) {
  auto s = __builtin_amdgcn_permlane16_swap(__float_as_uint(x), __float_as_uint(x), false, false);
  x = fmaxf(__uint_as_float(s[0]), __uint_as_float(s[1]));
  auto t = __builtin_amdgcn_permlane32_swap(__float_as_uint(x), __float_as_uint(x), false, false);
  return fmaxf(__uint_as_float(t[0]), __uint_as_float(t[1]));
}
DI float xg_sum(float x) {
  auto s = __builtin_amdgcn_permlane16_swap(__float_as_uint(x), __float_as_uint(x), false, false);
  x = __uint_as_float(s[0]) + __uint_as_float(s[1]);
  auto t = __builtin_amdgcn_permlane32_swap(__float_as_uint(x), __float_as_uint(x), false, false);
  return __uint_as_float(t[0]) + __uint_as_float(t[1]);
}

constexpr float SL2E = 0.088388347648318447f * 1.4426950408889634f;


__global__ __launch_bounds__(256, 2) void k1_qproj_attn(const char* __restrict__ xA, const char* __restrict__ kA, const char* __restrict__ vA,
                                                        const char* __restrict__ wqA, const char* __restrict__ xB, const char* __restrict__ kB,
                                                        const char* __restrict__ vB, const char* __restrict__ wqB, char* __restrict__ ows) {
  __shared__ __attribute__((aligned(1024))) char smem[81920];
  const unsigned lbase = (unsigned)(size_t)(LAS char*)smem;
  const int tid = threadIdx.x, l = tid & 63, w = __builtin_amdgcn_readfirstlane(tid >> 6);
  const int xcd = blockIdx.x & 7, j = blockIdx.x >> 3;
  const int br = (j ^ (j >> 5)) & 1, idx = j >> 1;
  int head, pair, kvh, hkv;
  if (br) { head = 4 * xcd + (idx >> 3); pair = idx & 7; kvh = head; hkv = 32; }
  else { kvh = xcd >> 1; head = 8 * kvh + (idx >> 2); pair = (xcd & 1) * 4 + (idx & 3); hkv = 4; }
  const char* xg = (br ? xB : xA) + (size_t)pair * 64 * 2048;
  const char* wq = (br ? wqB : wqA) + head * 256;
  const int rot = (2 * pair + head) & 15;
  const int c = l & 15, g = l >> 4, q4 = c >> 2, p = l & 3;
  const int u = w >> 1, dh = w & 1;

  const unsigned vw0 = (unsigned)(g * 8192 + ((c ^ (2 * g)) * 16));
  const unsigned vw1 = (unsigned)(g * 8192 + ((c ^ (2 * g) ^ 8) * 16));
  const unsigned vx0 = (unsigned)((l >> 3) * 2048 + (((l & 7) ^ g) * 16));
  const unsigned vx1 = (unsigned)((l >> 3) * 2048 + (((l & 7) ^ g ^ 4) * 16));
  const char* wq_w = wq + (size_t)(16 * w) * 8192;
  const char* xg_w = xg + (size_t)(16 * w) * 2048;
  auto stage_issue = [&](int s0, int buf) {
    const int s = (s0 + rot) & 15;
    const char* wb = wq_w + (size_t)s * (64 * 8192);
    const char* xb = xg_w + s * 128;
    const unsigned lw = lbase + buf * 16384 + w * 4096;
    const unsigned lx = lbase + 49152 + buf * 8192 + w * 2048;
    glds<0, true>(wb, vw0, lw);
    glds<0, true>(wb + 4 * 8192, vw0, lw + 1024);
    glds<0, true>(wb + 8 * 8192, vw1, lw + 2048);
    glds<0, true>(wb + 12 * 8192, vw1, lw + 3072);
    glds<0, false>(xb, vx0, lx);
    glds<0, false>(xb + 8 * 2048, vx1, lx + 1024);
  };
  stage_issue(0, 0);
  stage_issue(1, 1);

  const int b = 2 * pair + u;
  const size_t kvoff = ((size_t)(b * hkv + kvh) * 512 + dh * 256) * 256;
  const char* kg = (br ? kB : kA) + kvoff;
  const char* vg = (br ? vB : vA) + kvoff;
  unsigned vk[4], vv[2];
#pragma unroll
  for (int i = 0; i < 4; ++i) vk[i] = (unsigned)(g * 256 + ((c ^ g ^ (4 * i)) * 16));
#pragma unroll
  for (int i = 0; i < 2; ++i) vv[i] = (unsigned)(g * 256 + ((c ^ (2 * g) ^ (8 * i)) * 16));
  auto slot_off = [](int n) constexpr { return ((n & 3) == 0 ? 1 : (n & 3) == 1 ? 2 : (n & 3) == 2 ? 0 : 3) * 16384; };
  auto tile_issue = [&](int n) {
    const int blk = n >> 3, isV = (n >> 2) & 1, t = n & 3;
    const unsigned la = lbase + slot_off(n) + w * 4096;
    const char* src = (isV ? vg : kg) + (blk * 4 + t) * 4096;
    if (isV) glds4<true>(src, vv[0], vv[1], vv[0], vv[1], la);
    else glds4<true>(src, vk[0], vk[1], vk[2], vk[3], la);
  };

  unsigned aw[4];
  {
    const int F = 2 * q4 | 8 * (g & 1);
#pragma unroll
    for (int t = 0; t < 4; ++t) aw[t] = (unsigned)((8 * g + q4) * 256 + ((((2 * (4 * dh + t)) ^ F) + (p >> 1)) * 16) + (p & 1) * 8);
  }
  const unsigned ax0 = (unsigned)(49152 + (32 * u + c) * 128 + ((g ^ (c >> 1)) * 16));
  const unsigned ax1 = ax0 ^ 64;

  f32x4 qa[4][2];
#pragma unroll
  for (int t = 0; t < 4; ++t) { qa[t][0] = f32x4{0, 0, 0, 0}; qa[t][1] = f32x4{0, 0, 0, 0}; }
#pragma unroll
  for (int s = 0; s < 16; ++s) {
    const int buf = s % 3;
    if (s < 15) WAIT_V(6); else WAIT_V(4);
    WAIT_L0();
    BAR();
    if (s + 2 < 16) stage_issue(s + 2, (s + 2) % 3);
    else tile_issue(s - 14);
#pragma unroll
    for (int ks = 0; ks < 2; ++ks) {
      bf16x8 bx[2], af[4];
#pragma unroll
      for (int v = 0; v < 2; ++v) bx[v] = lds_b128(smem, (ks ? ax1 : ax0) + buf * 8192 + v * 2048);
#pragma unroll
      for (int t = 0; t < 4; ++t) af[t] = cat8(lds_tr(smem, aw[t] + buf * 16384 + ks * 8192), lds_tr(smem, aw[t] + buf * 16384 + ks * 8192 + 1024));
#pragma unroll
      for (int t = 0; t < 4; ++t)
#pragma unroll
        for (int v = 0; v < 2; ++v) qa[t][v] = mfma16(af[t], bx[v], qa[t][v]);
    }
  }
#pragma unroll
  for (int t = 0; t < 4; ++t)
#pragma unroll
    for (int v = 0; v < 2; ++v) {
      const int tau = 4 * dh + t;
      u32x2 pkd = {pk2(qa[t][v][0], qa[t][v][1]), pk2(qa[t][v][2], qa[t][v][3])};
      const unsigned off = 65536 + u * 8192 + v * 4096 + (tau >> 1) * 1024 + ((2 * (tau & 1) + (g >> 1)) * 16 + c) * 16 + (g & 1) * 8;
      *(u32x2*)(smem + off) = pkd;
    }
  WAIT_L0();
  BAR();
  tile_issue(2);
  tile_issue(3);
  bf16x8 qf[2][4];
#pragma unroll
  for (int v = 0; v < 2; ++v)
#pragma unroll
    for (int s = 0; s < 4; ++s) qf[v][s] = lds_b128(smem, 65536 + u * 8192 + v * 4096 + s * 1024 + l * 16);

  unsigned ak[4], av[8];
#pragma unroll
  for (int s = 0; s < 4; ++s) ak[s] = (unsigned)(w * 4096 + c * 256 + ((g ^ c ^ (4 * s)) * 16));
  {
    const int row = 4 * g + q4, F = 2 * (row & 7);
#pragma unroll
    for (int t = 0; t < 8; ++t) av[t] = (unsigned)(w * 4096 + row * 256 + ((((2 * t) ^ F) + (p >> 1)) * 16) + (p & 1) * 8);
  }
  f32x4 o[8][2];
#pragma unroll
  for (int t = 0; t < 8; ++t) { o[t][0] = f32x4{0, 0, 0, 0}; o[t][1] = f32x4{0, 0, 0, 0}; }
  float m[2] = {0.f, 0.f}, ls[2] = {0.f, 0.f};
#pragma unroll
  for (int blk = 0; blk < 4; ++blk) {
    f32x4 sc[4][2];
#pragma unroll
    for (int t = 0; t < 4; ++t) {
      const int n = blk * 8 + t;
      WAIT_V(12);
      bf16x8 kf[4];
#pragma unroll
      for (int s = 0; s < 4; ++s) kf[s] = lds_b128(smem, ak[s] + slot_off(n));
      WAIT_L0();
      tile_issue(n + 4);
      sc[t][0] = f32x4{0, 0, 0, 0}; sc[t][1] = f32x4{0, 0, 0, 0};
#pragma unroll
      for (int s = 0; s < 4; ++s)
#pragma unroll
        for (int v = 0; v < 2; ++v) sc[t][v] = mfma16(kf[s], qf[v][s], sc[t][v]);
      SB();
    }
    bf16x8 pf[2][2];
#pragma unroll
    for (int v = 0; v < 2; ++v) {
      float mx = sc[0][v][0];
#pragma unroll
      for (int t = 0; t < 4; ++t)
#pragma unroll
        for (int i = 0; i < 4; ++i) mx = fmaxf(mx, sc[t][v][i]);
      mx = xg_max(mx) * SL2E;
      float mn;
      if (blk == 0) { mn = mx; }
      else {
        mn = fmaxf(m[v], mx);
        const float alpha = __builtin_amdgcn_exp2f(m[v] - mn);
        ls[v] *= alpha;
#pragma unroll
        for (int t = 0; t < 8; ++t) o[t][v] *= alpha;
      }
      m[v] = mn;
      float sum = 0.f;
#pragma unroll
      for (int t = 0; t < 4; ++t)
#pragma unroll
        for (int i = 0; i < 4; ++i) { const float e = __builtin_amdgcn_exp2f(fmaf(sc[t][v][i], SL2E, -mn)); sc[t][v][i] = e; sum += e; }
      ls[v] += sum;
#pragma unroll
      for (int kb = 0; kb < 2; ++kb) {
        u32x4 pw = {pk2(sc[2 * kb][v][0], sc[2 * kb][v][1]), pk2(sc[2 * kb][v][2], sc[2 * kb][v][3]),
                    pk2(sc[2 * kb + 1][v][0], sc[2 * kb + 1][v][1]), pk2(sc[2 * kb + 1][v][2], sc[2 * kb + 1][v][3])};
        pf[kb][v] = __builtin_bit_cast(bf16x8, pw);
      }
    }
#pragma unroll
    for (int kb = 0; kb < 2; ++kb) {
      s16x4 vlo[8], vhi[8];
      const int n = blk * 8 + 4 + 2 * kb;
      if (n + 3 < 32) WAIT_V(12); else WAIT_V(4);
#pragma unroll
      for (int t = 0; t < 8; ++t) vlo[t] = lds_tr(smem, av[t] + slot_off(n));
      WAIT_L0();
      if (n + 4 < 32) tile_issue(n + 4);
      if (n + 4 < 32) WAIT_V(12); else if (n + 1 == 29) WAIT_V(8); else WAIT_V(0);
#pragma unroll
      for (int t = 0; t < 8; ++t) vhi[t] = lds_tr(smem, av[t] + slot_off(n + 1));
      WAIT_L0();
      if (n + 5 < 32) tile_issue(n + 5);
#pragma unroll
      for (int t = 0; t < 8; ++t)
#pragma unroll
        for (int v = 0; v < 2; ++v) o[t][v] = mfma16(cat8(vlo[t], vhi[t]), pf[kb][v], o[t][v]);
      SB();
    }
  }
  ls[0] = xg_sum(ls[0]);
  ls[1] = xg_sum(ls[1]);
  if (dh) {
#pragma unroll
    for (int t = 0; t < 8; ++t) { const f32x4 tmp = o[t][0]; o[t][0] = o[t][1]; o[t][1] = tmp; }
    float tm = m[0]; m[0] = m[1]; m[1] = tm;
    tm = ls[0]; ls[0] = ls[1]; ls[1] = tm;
  }
  {
#pragma unroll
    for (int t = 0; t < 8; ++t) *(f32x4*)(smem + 16384 + (t >> 2) * 16384 + w * 4096 + (t & 3) * 1024 + l * 16) = o[t][1];
    f32x2 ml = {m[1], ls[1]};
    *(f32x2*)(smem + w * 4096 + l * 8) = ml;
  }
  WAIT_L0();
  BAR();
  {
    const int pw = w ^ 1;
    const f32x2 ml = *(const f32x2*)(smem + pw * 4096 + l * 8);
    const float mo = m[0], lo = ls[0];
    const float mm = fmaxf(mo, ml[0]);
    float a0 = __builtin_amdgcn_exp2f(mo - mm), a1 = __builtin_amdgcn_exp2f(ml[0] - mm);
    const float inv = 1.0f / (lo * a0 + ml[1] * a1);
    a0 *= inv; a1 *= inv;
    char* ob = ows + (size_t)br * (4u << 20) + ((size_t)(2 * b + dh) * 128 + head * 4) * 1024;
#pragma unroll
    for (int t = 0; t < 8; ++t) {
      const f32x4 oth = *(const f32x4*)(smem + 16384 + (t >> 2) * 16384 + pw * 4096 + (t & 3) * 1024 + l * 16);
      const f32x4 r = o[t][0] * a0 + oth * a1;
      u32x2 pkd = {pk2(r[0], r[1]), pk2(r[2], r[3])};
      *(u32x2*)(ob + (t >> 1) * 1024 + ((2 * (t & 1) + (g >> 1)) * 16 + c) * 16 + (g & 1) * 8) = pkd;
    }
  }
}

__global__ __launch_bounds__(256, 1) void k2_oproj(const char* __restrict__ ows, const char* __restrict__ woA, const char* __restrict__ woB,
                                                   char* __restrict__ out) {
  __shared__ __attribute__((aligned(1024))) char smem[163840];
  const unsigned lbase = (unsigned)(size_t)(LAS char*)smem;
  const int tid = threadIdx.x, l = tid & 63, w = __builtin_amdgcn_readfirstlane(tid >> 6);
  const int xcd = blockIdx.x & 7, j = blockIdx.x >> 3;
  const int br = xcd >> 2, nt = (xcd & 3) * 4 + (j & 3), mt = j >> 2;
  const int c = l & 15, g = l >> 4, q4 = c >> 2, p = l & 3;
  const int rot = (mt * 4 + (j & 3)) & 31;
  const char* og = ows + (size_t)br * (4u << 20) + ((size_t)(mt * 4) * 128 + w * 32) * 1024;
  const char* wo = (br ? woB : woA) + (size_t)(w * 1024) * 2048 + nt * 128;
  const unsigned vo = l * 16;
  const unsigned vw0 = (unsigned)((l >> 3) * 2048 + (((l & 7) ^ (2 * (g & 1))) * 16));
  const unsigned vw1 = (unsigned)((l >> 3) * 2048 + (((l & 7) ^ (2 * (g & 1)) ^ 4) * 16));
  constexpr int R = 5;
  const unsigned lw = lbase + w * (R * 8192);
  auto issue = [&](int s0, int slot) {
    const int s = (s0 + rot) & 31;
    const char* ob = og + s * 1024;
    const char* wb = wo + (size_t)s * (32 * 2048);
    const unsigned la = lw + slot * 8192;
    glds<0, true>(wb, vw0, la);
    glds<0, true>(wb + 8 * 2048, vw1, la + 1024);
    glds<0, true>(wb + 16 * 2048, vw0, la + 2048);
    glds<0, true>(wb + 24 * 2048, vw1, la + 3072);
    glds<0, false>(ob, vo, la + 4096);
    glds<0, false>(ob + 128 * 1024, vo, la + 5120);
    glds<0, false>(ob + 256 * 1024, vo, la + 6144);
    glds<0, false>(ob + 384 * 1024, vo, la + 7168);
  };
#pragma unroll
  for (int s = 0; s < R; ++s) issue(s, s);
  unsigned aw[4];
  {
    const int F = 2 * ((q4 >> 1) | ((g & 1) << 1));
#pragma unroll
    for (int t = 0; t < 4; ++t) aw[t] = (unsigned)(w * (R * 8192) + (8 * g + q4) * 128 + ((((2 * t) ^ F) + (p >> 1)) * 16) + (p & 1) * 8);
  }
  const unsigned ao = w * (R * 8192) + 4096 + l * 16;
  f32x4 acc[4][4];
#pragma unroll
  for (int t = 0; t < 4; ++t)
#pragma unroll
    for (int v = 0; v < 4; ++v) acc[t][v] = f32x4{0, 0, 0, 0};
#pragma unroll
  for (int s = 0; s < 32; ++s) {
    const int slot = s % R;
    const int rem = 31 - s;
    if (rem >= R - 1) WAIT_V(8 * (R - 1));
    else if (rem == 3) WAIT_V(24);
    else if (rem == 2) WAIT_V(16);
    else if (rem == 1) WAIT_V(8);
    else WAIT_V(0);
    bf16x8 bo[4], af[4];
#pragma unroll
    for (int v = 0; v < 4; ++v) bo[v] = lds_b128(smem, ao + slot * 8192 + v * 1024);
#pragma unroll
    for (int t = 0; t < 4; ++t) af[t] = cat8(lds_tr(smem, aw[t] + slot * 8192), lds_tr(smem, aw[t] + slot * 8192 + 512));
    WAIT_L0();
    if (s + R < 32) issue(s + R, slot);
#pragma unroll
    for (int t = 0; t < 4; ++t)
#pragma unroll
      for (int v = 0; v < 4; ++v) acc[t][v] = mfma16(af[t], bo[v], acc[t][v]);
  }
#pragma unroll
  for (int v = 0; v < 4; ++v)
#pragma unroll
    for (int t = 0; t < 4; ++t) *(f32x4*)(smem + w * (R * 8192) + (v * 4 + t) * 1024 + l * 16) = acc[t][v];
  WAIT_L0();
  BAR();
  char* ob = out + ((size_t)(mt * 64 + w * 16 + c) * 2048 + br * 1024 + nt * 64) * 2;
#pragma unroll
  for (int t = 0; t < 4; ++t) {
    f32x4 r = f32x4{0, 0, 0, 0};
#pragma unroll
    for (int sw = 0; sw < 4; ++sw) r += *(const f32x4*)(smem + sw * (R * 8192) + (w * 4 + t) * 1024 + l * 16);
    u32x2 pkd = {pk2(r[0], r[1]), pk2(r[2], r[3])};
    *(u32x2*)(ob + (16 * t + 4 * g) * 2) = pkd;
  }
}

extern "C" void kernel_launch(void* const* d_in, const int* in_sizes, int n_in, void* d_out, int out_size, void* d_ws, size_t ws_size,
                              hipStream_t stream) {
  const char* xA = (const char*)d_in[0];
  const char* kA = (const char*)d_in[1];
  const char* vA = (const char*)d_in[2];
  const char* wqA = (const char*)d_in[3];
  const char* woA = (const char*)d_in[4];
  const char* xB = (const char*)d_in[5];
  const char* kB = (const char*)d_in[6];
  const char* vB = (const char*)d_in[7];
  const char* wqB = (const char*)d_in[8];
  const char* woB = (const char*)d_in[9];
  char* ows = (char*)d_ws;
  k1_qproj_attn<<<512, 256, 0, stream>>>(xA, kA, vA, wqA, xB, kB, vB, wqB, ows);
  k2_oproj<<<256, 256, 0, stream>>>(ows, woA, woB, (char*)d_out);
}
```

```cpp
#include <hip/hip_runtime.h>
#include <hip/hip_bf16.h>

#define POL_Q 2
#define POL_KVA 2

typedef short bf16x8 __attribute__((ext_vector_type(8)));
typedef short s16x4 __attribute__((ext_vector_type(4)));
typedef float f32x4 __attribute__((ext_vector_type(4)));
typedef float f32x2 __attribute__((ext_vector_type(2)));
typedef __bf16 bf16x2 __attribute__((ext_vector_type(2)));
typedef unsigned u32x4 __attribute__((ext_vector_type(4)));
typedef unsigned u32x2 __attribute__((ext_vector_type(2)));
#define LAS __attribute__((address_space(3)))
#define DI __device__ __forceinline__

#define WAIT_V(n) asm volatile("s_waitcnt vmcnt(%0)" ::"n"(n) : "memory")
#define WAIT_L0() do { asm volatile("" ::: "memory"); __builtin_amdgcn_s_waitcnt(0xC07F); asm volatile("" ::: "memory"); } while (0)
#define BAR() __builtin_amdgcn_s_barrier()
#define SB() __builtin_amdgcn_sched_barrier(0)

template <bool NT>
DI void glds(const char* sbase, unsigned voff, unsigned ldsaddr) {
  if (NT)
    asm volatile("s_mov_b32 m0, %2\n\ts_nop 0\n\tglobal_load_lds_dwordx4 %0, %1 nt" ::"v"(voff), "s"(sbase), "s"(ldsaddr) : "memory");
  else
    asm volatile("s_mov_b32 m0, %2\n\ts_nop 0\n\tglobal_load_lds_dwordx4 %0, %1" ::"v"(voff), "s"(sbase), "s"(ldsaddr) : "memory");
}
template <bool NT>
DI void glds4(const char* sbase, unsigned v0, unsigned v1, unsigned v2, unsigned v3, unsigned ldsaddr) {
  if (NT)
    asm volatile("s_mov_b32 m0, %5\n\ts_nop 0\n\t"
                 "global_load_lds_dwordx4 %0, %4 nt\n\t"
                 "global_load_lds_dwordx4 %1, %4 offset:1024 nt\n\t"
                 "global_load_lds_dwordx4 %2, %4 offset:2048 nt\n\t"
                 "global_load_lds_dwordx4 %3, %4 offset:3072 nt"
                 ::"v"(v0), "v"(v1), "v"(v2), "v"(v3), "s"(sbase), "s"(ldsaddr) : "memory");
  else
    asm volatile("s_mov_b32 m0, %5\n\ts_nop 0\n\t"
                 "global_load_lds_dwordx4 %0, %4\n\t"
                 "global_load_lds_dwordx4 %1, %4 offset:1024\n\t"
                 "global_load_lds_dwordx4 %2, %4 offset:2048\n\t"
                 "global_load_lds_dwordx4 %3, %4 offset:3072"
                 ::"v"(v0), "v"(v1), "v"(v2), "v"(v3), "s"(sbase), "s"(ldsaddr) : "memory");
}
DI void pin(const char* sbase, unsigned voff, unsigned ldsaddr) {
  asm volatile("s_mov_b32 m0, %2\n\ts_nop 0\n\tglobal_load_lds_dword %0, %1" ::"v"(voff), "s"(sbase), "s"(ldsaddr) : "memory");
}

DI f32x4 mfma16(bf16x8 a, bf16x8 b, f32x4 c) { return __builtin_amdgcn_mfma_f32_16x16x32_bf16(a, b, c, 0, 0, 0); }
DI unsigned pk2(float lo, float hi) { f32x2 a = {lo, hi}; return __builtin_bit_cast(unsigned, __builtin_convertvector(a, bf16x2)); }
DI bf16x8 lds_b128(const char* smem, unsigned off) { return *(const bf16x8*)(smem + off); }
DI s16x4 lds_tr(char* smem, unsigned off) { return __builtin_amdgcn_ds_read_tr16_b64_v4i16((LAS s16x4*)(smem + off)); }
DI bf16x8 cat8(s16x4 lo, s16x4 hi) { return __builtin_shufflevector(lo, hi, 0, 1, 2, 3, 4, 5, 6, 7); }
DI float xg_max(float x) {
  auto s = __builtin_amdgcn_permlane16_swap(__float_as_uint(x), __float_as_uint(x), false, false);
  x = fmaxf(__uint_as_float(s[0]), __uint_as_float(s[1]));
  auto t = __builtin_amdgcn_permlane32_swap(__float_as_uint(x), __float_as_uint(x), false, false);
  return fmaxf(__uint_as_float(t[0]), __uint_as_float(t[1]));
}
DI float xg_sum(float x) {
  auto s = __builtin_amdgcn_permlane16_swap(__float_as_uint(x), __float_as_uint(x), false, false);
  x = __uint_as_float(s[0]) + __uint_as_float(s[1]);
  auto t = __builtin_amdgcn_permlane32_swap(__float_as_uint(x), __float_as_uint(x), false, false);
  return __uint_as_float(t[0]) + __uint_as_float(t[1]);
}

#define STAMP(k)
#define STAMP_DECL
#define STAMP_FLUSH(ws)
constexpr float SL2E = 0.088388347648318447f * 1.4426950408889634f;

#define ASLEEP 0

#define G8_(j, off, sfx) "global_load_lds_dwordx4 %" #j ", %8 offset:" #off sfx "\n\t"
template <int MODE>
DI void glds8(const char* sbase, const unsigned (&v)[8], unsigned ldsaddr) {
  if (MODE == 0)
    asm volatile("s_mov_b32 m0, %9\n\ts_nop 0\n\t" G8_(0, -4096, "") G8_(1, -3072, "") G8_(2, -2048, "") G8_(3, -1024, "") G8_(4, 0, "") G8_(5, 1024, "") G8_(6, 2048, "") G8_(7, 3072, "")
                 ::"v"(v[0]), "v"(v[1]), "v"(v[2]), "v"(v[3]), "v"(v[4]), "v"(v[5]), "v"(v[6]), "v"(v[7]), "s"(sbase), "s"(ldsaddr) : "memory");
  else if (MODE == 1)
    asm volatile("s_mov_b32 m0, %9\n\ts_nop 0\n\t" G8_(0, -4096, " nt") G8_(1, -3072, " nt") G8_(2, -2048, " nt") G8_(3, -1024, " nt") G8_(4, 0, " nt") G8_(5, 1024, " nt") G8_(6, 2048, " nt") G8_(7, 3072, " nt")
                 ::"v"(v[0]), "v"(v[1]), "v"(v[2]), "v"(v[3]), "v"(v[4]), "v"(v[5]), "v"(v[6]), "v"(v[7]), "s"(sbase), "s"(ldsaddr) : "memory");
  else
    asm volatile("s_mov_b32 m0, %9\n\ts_nop 0\n\t" G8_(0, -4096, " nt") G8_(0, -4096, "") G8_(1, -3072, " nt") G8_(1, -3072, "") G8_(2, -2048, " nt") G8_(2, -2048, "") G8_(3, -1024, " nt") G8_(3, -1024, "")
                 G8_(4, 0, " nt") G8_(4, 0, "") G8_(5, 1024, " nt") G8_(5, 1024, "") G8_(6, 2048, " nt") G8_(6, 2048, "") G8_(7, 3072, " nt") G8_(7, 3072, "")
                 ::"v"(v[0]), "v"(v[1]), "v"(v[2]), "v"(v[3]), "v"(v[4]), "v"(v[5]), "v"(v[6]), "v"(v[7]), "s"(sbase), "s"(ldsaddr) : "memory");
}
#define CVAR 1
#define RPG 2
#define H0SLEEP 12
#define KVAPOL 0
#define BLKBAR 0
#define BLKBAR2 0
#define FDUP 1
#define GSARG
#define GSPASS
#define GBAR() BAR()
struct HFrags { s16x4 a[4][2]; bf16x8 b[4]; };
template <int WS, int XT, bool TAIL, class TailF>
DI void gemm_core(char* smem, unsigned lbase, const char* wg, const char* xg, int rot, int w, int l, f32x4 (&acc)[4][4], TailF tail GSARG) {
  int bn_ = 1; (void)bn_;
  const int c = l & 15, g = l >> 4, q4 = c >> 2, p = l & 3;
  if (w >= 4) {
    const int iw = w & 1;
    unsigned vw[8], vx[8];
    {
      const unsigned vw0 = (unsigned)(g * WS + ((c ^ (2 * g)) * 16)), vw1 = vw0 ^ 128;
      const unsigned vx0 = XT ? (unsigned)(l * 16) : (unsigned)((l >> 3) * 2048 + (((l & 7) ^ g) * 16));
      const unsigned vx1 = XT ? vx0 : (vx0 ^ 64);
#pragma unroll
      for (int j = 0; j < 8; ++j) {
        vw[j] = ((j & 2) ? vw1 : vw0) + j * (4 * WS - 1024);
        vx[j] = XT ? vx0 + (j >> 1) * (128 * 1024 - 2048) : ((j & 1) ? vx1 : vx0) + j * (16384 - 1024);
      }
    }
    const char* w_w = wg + (size_t)(32 * iw) * WS + 4096;
    const char* x_w = (XT ? xg + (size_t)(4 * iw) * (128 * 1024) : xg + (size_t)(64 * iw) * 2048) + 4096;
    const unsigned lw0 = lbase + iw * 8192 + 4096;
    auto wpieces = [&](int kap, unsigned bufoff, bool first) {
      if (first) glds8<FDUP ? 2 : 1>(w_w + (size_t)kap * (64 * WS), vw, lw0 + bufoff);
      else glds8<0>(w_w + (size_t)kap * (64 * WS), vw, lw0 + bufoff);
    };
    auto xpieces = [&](int kap, unsigned bufoff) { glds8<0>(x_w + kap * (XT ? 2048 : 128), vx, lw0 + bufoff + 16384); };
    constexpr int FN = FDUP ? 24 : 16;
    if (w < 6) {
#pragma unroll 1
      for (int f = 0; f < 2; ++f) { wpieces(rot + f, f * 32768, true); xpieces(rot + f, f * 32768); }
#pragma unroll 1
      for (int i = 0; i < 2; ++i) {
        WAIT_V(FN);
        GBAR();
        GBAR();
        wpieces(rot + i + 2, i * 32768, true); xpieces(rot + i + 2, i * 32768);
        GBAR();
        GBAR();
      }
      WAIT_V(FN);
      GBAR();
      GBAR();
      if (TAIL) tail(0);
      GBAR();
      if (TAIL) tail(1);
      GBAR();
      if (TAIL) tail(2);
      WAIT_V(TAIL ? 12 : 0);
      GBAR();
      if (TAIL) tail(3);
      GBAR();
      GBAR();
      GBAR();
    } else {
      auto stage = [&](int kap, int hb) { wpieces(kap & 15, 65536 + hb * 32768, false); xpieces(kap & 15, 65536 + hb * 32768); };
      __builtin_amdgcn_s_sleep(H0SLEEP);
      stage(rot + 4, 0);
      GBAR();
      stage(rot + 8, 1);
      WAIT_V(16);
      GBAR();
      stage(rot + 12, 2);
#pragma unroll 1
      for (int i = 0; i < 3; ++i) {
        WAIT_V(16);
        GBAR();
        stage(rot + 5 + i, 0);
        WAIT_V(16);
        GBAR();
        stage(rot + 9 + i, 1);
        GBAR();
        stage(rot + 13 + i, 2);
        WAIT_V(32);
        GBAR();
      }
      if (TAIL) { tail(0); tail(1); }
      WAIT_V(TAIL ? 24 : 16);
      GBAR();
      if (TAIL) { tail(2); tail(3); }
      WAIT_V(TAIL ? 16 : 0);
      GBAR();
    }
  } else {
    const int nh = w & 1, mh = w >> 1;
    unsigned aw[4];
    {
      const int F = 2 * q4 | 8 * (g & 1);
#pragma unroll
      for (int t = 0; t < 4; ++t) aw[t] = (unsigned)((8 * g + q4) * 256 + ((((2 * (4 * nh + t)) ^ F) + (p >> 1)) * 16) + (p & 1) * 8);
    }
    const unsigned ax0 = XT ? (unsigned)(16384 + mh * 8192 + l * 16) : (unsigned)(16384 + (64 * mh + c) * 128 + ((g ^ (c >> 1)) * 16));
    const unsigned ax1 = XT ? ax0 + 1024 : (ax0 ^ 64);
    struct FF { HFrags h[2]; };
    auto mm = [&](const FF& fo) {
#pragma unroll
      for (int ks = 0; ks < 2; ++ks)
#pragma unroll
        for (int t = 0; t < 4; ++t)
#pragma unroll
          for (int v = 0; v < 4; ++v) acc[t][v] = mfma16(cat8(fo.h[ks].a[t][0], fo.h[ks].a[t][1]), fo.h[ks].b[v], acc[t][v]);
    };
    auto full = [&](FF& fn, const FF& fo, unsigned bo) {
#pragma unroll
      for (int ks = 0; ks < 2; ++ks)
#pragma unroll
        for (int t = 0; t < 4; ++t) {
          fn.h[ks].b[t] = lds_b128(smem, (ks ? ax1 : ax0) + bo + t * 2048);
          fn.h[ks].a[t][0] = lds_tr(smem, aw[t] + bo + ks * 8192);
          fn.h[ks].a[t][1] = lds_tr(smem, aw[t] + bo + ks * 8192 + 1024);
        }
      mm(fo);
#pragma unroll
      for (int t = 0; t < 24; ++t) {
        __builtin_amdgcn_sched_group_barrier(0x008, 1, 0);
        __builtin_amdgcn_sched_group_barrier(0x100, 1, 0);
      }
      __builtin_amdgcn_sched_group_barrier(0x008, 8, 0);
      SB();
    };
#pragma unroll
    for (int t = 0; t < 4; ++t)
#pragma unroll
      for (int v = 0; v < 4; ++v) acc[t][v] = f32x4{0, 0, 0, 0};
    FF f0, f1;
#pragma unroll
    for (int ks = 0; ks < 2; ++ks)
#pragma unroll
      for (int t = 0; t < 4; ++t) { f1.h[ks].b[t] = bf16x8{0, 0, 0, 0, 0, 0, 0, 0}; f1.h[ks].a[t][0] = s16x4{0, 0, 0, 0}; f1.h[ks].a[t][1] = s16x4{0, 0, 0, 0}; }
#pragma unroll 1
    for (int n = 0; n < 16; n += 2) {
      const unsigned bo0 = (n & 2) ? 98304 : ((n >> 2) & 1) * 32768, bo1 = (n & 2) ? 131072 : 65536;
      WAIT_L0();
      GBAR();
      full(f0, f1, bo0);
      WAIT_L0();
      GBAR();
      full(f1, f0, bo1);
    }
    WAIT_L0();
    mm(f1);
  }
}

__global__ __launch_bounds__(512, 2) void k1_qproj_attn(const char* __restrict__ xA, const char* __restrict__ kA, const char* __restrict__ vA,
                                                        const char* __restrict__ wqA, const char* __restrict__ xB, const char* __restrict__ kB,
                                                        const char* __restrict__ vB, const char* __restrict__ wqB, char* __restrict__ ows) {
  __shared__ __attribute__((aligned(1024))) char smem[163840];
  const unsigned lbase = (unsigned)(size_t)(LAS char*)smem;
  const int tid = threadIdx.x, l = tid & 63, w = __builtin_amdgcn_readfirstlane(tid >> 6);
  const int xcd = blockIdx.x & 7, j = blockIdx.x >> 3;
  const int br = j & 1, idx = j >> 1;
  int head, bg, kvh, hkv, rot, brot;
  if (br) { const int a = idx >> 2; bg = idx & 3; head = 4 * xcd + a; kvh = head; hkv = 32; rot = 4 * ((a + bg) & 3); brot = 0; }
  else { const int a = idx >> 1; kvh = xcd >> 1; head = 8 * kvh + a; bg = (xcd & 1) * 2 + (idx & 1); hkv = 4; rot = 4 * ((a + 2 * (idx & 1)) & 3); brot = a & 3; }
  const char* xg = (br ? xB : xA) + (size_t)bg * 128 * 2048;
  const char* wq = (br ? wqB : wqA) + head * 256;
  const int c = l & 15, g = l >> 4, q4 = c >> 2, p = l & 3;
  const int u = w & 3, dh = w >> 2;
  const unsigned ring = (unsigned)((w >= 4 ? w - 4 : w < 2 ? w + 4 : w + 6) * 16384);
  STAMP_DECL;
  STAMP(0);
  if (ASLEEP && !br) {
#pragma unroll 1
    for (int i = 0; i < ASLEEP; ++i) __builtin_amdgcn_s_sleep(16);
  }
  const int b = 4 * bg + u;
  const size_t kvoff = ((size_t)(b * hkv + kvh) * 512 + dh * 256) * 256;
  const char* kg = (br ? kB : kA) + kvoff;
  const char* vg = (br ? vB : vA) + kvoff;
  unsigned vk[4], vv[2];
#pragma unroll
  for (int i = 0; i < 4; ++i) vk[i] = (unsigned)(g * 256 + ((c ^ g ^ (4 * i)) * 16));
#pragma unroll
  for (int i = 0; i < 2; ++i) vv[i] = (unsigned)(g * 256 + ((c ^ (2 * g) ^ (8 * i)) * 16));
  auto slot_off = [](int t) constexpr { return t * 4096; };
  auto tile_issue = [&](int blk, int isV, int t) {
    const unsigned la = lbase + slot_off(t) + ring;
    const char* src = (isV ? vg : kg) + (((blk + brot) & 3) * 4 + t) * 4096;
    if (br) {
      if (isV) glds4<true>(src, vv[0], vv[1], vv[0], vv[1], la);
      else glds4<true>(src, vk[0], vk[1], vk[2], vk[3], la);
    } else {
      if (KVAPOL && blk == 0 && (KVAPOL == 2 || idx < 8)) {
        if (isV) glds4<true>(src, vv[0], vv[1], vv[0], vv[1], la);
        else glds4<true>(src, vk[0], vk[1], vk[2], vk[3], la);
      }
      if (isV) glds4<false>(src, vv[0], vv[1], vv[0], vv[1], la);
      else glds4<false>(src, vk[0], vk[1], vk[2], vk[3], la);
    }
  };

  f32x4 qa[4][4];
  gemm_core<8192, 0, true>(smem, lbase, wq, xg, rot, w, l, qa, [&](int n) { tile_issue(0, 0, n); } GSPASS);
  if (w < 4) {
#pragma unroll
    for (int t = 0; t < 4; ++t)
#pragma unroll
      for (int v = 0; v < 4; ++v) {
        const int tau = 4 * (w & 1) + t;
        u32x2 pkd = {pk2(qa[t][v][0], qa[t][v][1]), pk2(qa[t][v][2], qa[t][v][3])};
        const unsigned off = 98304 + ((w >> 1) * 4 + v) * 4096 + (tau >> 1) * 1024 + ((2 * (tau & 1) + (g >> 1)) * 16 + c) * 16 + (g & 1) * 8;
        *(u32x2*)(smem + off) = pkd;
      }
  }
  WAIT_L0();
  BAR();
  bf16x8 qf[2][4];
#pragma unroll
  for (int v = 0; v < 2; ++v)
#pragma unroll
    for (int s = 0; s < 4; ++s) qf[v][s] = lds_b128(smem, 98304 + u * 8192 + v * 4096 + s * 1024 + l * 16);
  if (w < 4) {
#pragma unroll 1
    for (int t = 0; t < 4; ++t) tile_issue(0, 0, t);
  }
  STAMP(1);
#define STAMPA(k) STAMP(k)

  unsigned ak[4], av[8];
#pragma unroll
  for (int s = 0; s < 4; ++s) ak[s] = (unsigned)(ring + c * 256 + ((g ^ c ^ (4 * s)) * 16));
  {
    const int row = 4 * g + q4, F = 2 * (row & 7);
#pragma unroll
    for (int t = 0; t < 8; ++t) av[t] = (unsigned)(ring + row * 256 + ((((2 * t) ^ F) + (p >> 1)) * 16) + (p & 1) * 8);
  }
  f32x4 o[8][2];
#pragma unroll
  for (int t = 0; t < 8; ++t) { o[t][0] = f32x4{0, 0, 0, 0}; o[t][1] = f32x4{0, 0, 0, 0}; }
  float m[2] = {-__builtin_inff(), -__builtin_inff()}, ls[2] = {0.f, 0.f};
#pragma unroll 1
  for (int blk = 0; blk < 4; ++blk) {
    f32x4 sc[4][2];
#pragma unroll
    for (int t = 0; t < 4; ++t) {
      WAIT_V(12);
      bf16x8 kf[4];
#pragma unroll
      for (int s = 0; s < 4; ++s) kf[s] = lds_b128(smem, ak[s] + slot_off(t));
      WAIT_L0();
      tile_issue(blk, 1, t);
      sc[t][0] = f32x4{0, 0, 0, 0}; sc[t][1] = f32x4{0, 0, 0, 0};
#pragma unroll
      for (int s = 0; s < 4; ++s)
#pragma unroll
        for (int v = 0; v < 2; ++v) sc[t][v] = mfma16(kf[s], qf[v][s], sc[t][v]);
      SB();
    }
    bf16x8 pf[2][2];
#pragma unroll
    for (int v = 0; v < 2; ++v) {
      float mx = sc[0][v][0];
#pragma unroll
      for (int t = 0; t < 4; ++t)
#pragma unroll
        for (int i = 0; i < 4; ++i) mx = fmaxf(mx, sc[t][v][i]);
      mx = xg_max(mx) * SL2E;
      const float mn = fmaxf(m[v], mx);
      const float alpha = __builtin_amdgcn_exp2f(m[v] - mn);
      ls[v] *= alpha;
#pragma unroll
      for (int t = 0; t < 8; ++t) o[t][v] *= alpha;
      m[v] = mn;
      float sum = 0.f;
#pragma unroll
      for (int t = 0; t < 4; ++t)
#pragma unroll
        for (int i = 0; i < 4; ++i) { const float e = __builtin_amdgcn_exp2f(fmaf(sc[t][v][i], SL2E, -mn)); sc[t][v][i] = e; sum += e; }
      ls[v] += sum;
#pragma unroll
      for (int kb = 0; kb < 2; ++kb) {
        u32x4 pw = {pk2(sc[2 * kb][v][0], sc[2 * kb][v][1]), pk2(sc[2 * kb][v][2], sc[2 * kb][v][3]),
                    pk2(sc[2 * kb + 1][v][0], sc[2 * kb + 1][v][1]), pk2(sc[2 * kb + 1][v][2], sc[2 * kb + 1][v][3])};
        pf[kb][v] = __builtin_bit_cast(bf16x8, pw);
      }
    }
    const bool more = blk < 3;
    if (BLKBAR2 == 2 || (BLKBAR2 == 1 && !br)) BAR();
#pragma unroll
    for (int kb = 0; kb < 2; ++kb) {
      s16x4 vlo[8], vhi[8];
      if (kb == 0 || more) WAIT_V(12); else WAIT_V(4);
#pragma unroll
      for (int t = 0; t < 8; ++t) vlo[t] = lds_tr(smem, av[t] + slot_off(2 * kb));
      WAIT_L0();
      if (more) { tile_issue(blk + 1, 0, 2 * kb); WAIT_V(12); }
      else if (kb == 0) WAIT_V(8);
      else WAIT_V(0);
#pragma unroll
      for (int t = 0; t < 8; ++t) vhi[t] = lds_tr(smem, av[t] + slot_off(2 * kb + 1));
      WAIT_L0();
      if (more) tile_issue(blk + 1, 0, 2 * kb + 1);
#pragma unroll
      for (int t = 0; t < 8; ++t)
#pragma unroll
        for (int v = 0; v < 2; ++v) o[t][v] = mfma16(cat8(vlo[t], vhi[t]), pf[kb][v], o[t][v]);
      SB();
    }
    if (BLKBAR == 2 || (BLKBAR == 1 && !br)) BAR();
    STAMPA(2 + blk);
  }
  ls[0] = xg_sum(ls[0]);
  ls[1] = xg_sum(ls[1]);
  if (dh) {
#pragma unroll
    for (int t = 0; t < 8; ++t) { const f32x4 tmp = o[t][0]; o[t][0] = o[t][1]; o[t][1] = tmp; }
    float tm = m[0]; m[0] = m[1]; m[1] = tm;
    tm = ls[0]; ls[0] = ls[1]; ls[1] = tm;
  }
  {
#pragma unroll
    for (int t = 0; t < 8; ++t) *(f32x4*)(smem + ring + t * 1024 + l * 16) = o[t][1];
    f32x2 ml = {m[1], ls[1]};
    *(f32x2*)(smem + ring + 8192 + l * 8) = ml;
  }
  WAIT_L0();
  BAR();
  {
    const int pw = w ^ 4;
    const unsigned pring = (unsigned)((pw >= 4 ? pw - 4 : pw < 2 ? pw + 4 : pw + 6) * 16384);
    const f32x2 ml = *(const f32x2*)(smem + pring + 8192 + l * 8);
    const float mo = m[0], lo = ls[0];
    const float mm = fmaxf(mo, ml[0]);
    float a0 = __builtin_amdgcn_exp2f(mo - mm), a1 = __builtin_amdgcn_exp2f(ml[0] - mm);
    const float inv = 1.0f / (lo * a0 + ml[1] * a1);
    a0 *= inv; a1 *= inv;
    char* ob = ows + (size_t)br * (4u << 20) + ((size_t)(2 * b + dh) * 128 + head * 4) * 1024;
#pragma unroll
    for (int t = 0; t < 8; ++t) {
      const f32x4 oth = *(const f32x4*)(smem + pring + t * 1024 + l * 16);
      const f32x4 r = o[t][0] * a0 + oth * a1;
      u32x2 pkd = {pk2(r[0], r[1]), pk2(r[2], r[3])};
      *(u32x2*)(ob + (t >> 1) * 1024 + ((2 * (t & 1) + (g >> 1)) * 16 + c) * 16 + (g & 1) * 8) = pkd;
    }
  }
  STAMPA(6);
  STAMP_FLUSH(ows);
}

constexpr size_t PART_OFF = 16u << 20;
__global__ __launch_bounds__(512, 2) void k2_oproj(char* __restrict__ ows, const char* __restrict__ woA, const char* __restrict__ woB) {
  __shared__ __attribute__((aligned(1024))) char smem[163840];
  const unsigned lbase = (unsigned)(size_t)(LAS char*)smem;
  const int tid = threadIdx.x, l = tid & 63, w = __builtin_amdgcn_readfirstlane(tid >> 6);
  const int xcd = blockIdx.x & 7, j = blockIdx.x >> 3;
  const int br = xcd >> 2, kq = xcd & 3, mt = j & 3, nt = j >> 2;
  STAMP_DECL;
  STAMP(0);
  const char* og = ows + (size_t)br * (4u << 20) + ((size_t)(mt * 8) * 128 + kq * 32) * 1024;
  const char* wo = (br ? woB : woA) + (size_t)(kq * 1024) * 2048 + nt * 256;
  f32x4 acc[4][4];
  gemm_core<2048, 1, false>(smem, lbase, wo, og, 4 * ((mt + nt) & 3), w, l, acc, [](int) {} GSPASS);
  STAMP(1);
  if (w < 4) {
    char* pb = ows + PART_OFF + ((size_t)((br * 4 + kq) * 32 + mt * 8 + nt) * 4 + w) * 16384 + l * 16;
#pragma unroll
    for (int t = 0; t < 4; ++t)
#pragma unroll
      for (int v = 0; v < 4; ++v)
        *(f32x4*)(pb + (t * 4 + v) * 1024) = acc[t][v];
  }
  STAMP(2); STAMP(3); STAMP(4); STAMP(5); STAMP(6);
}
__global__ __launch_bounds__(256) void k3_reduce(const char* __restrict__ ows, char* __restrict__ out) {
  const int id = blockIdx.x * 256 + threadIdx.x;
  const int l = id & 63, v = (id >> 6) & 3, t = (id >> 8) & 3, w = (id >> 10) & 3, tile = (id >> 12) & 31, br = id >> 17;
  const char* pb = ows + PART_OFF + ((size_t)(br * 4 * 32 + tile) * 4096 + (id & 4095)) * 16;
  f32x4 r = *(const f32x4*)pb;
#pragma unroll
  for (int kq = 1; kq < 4; ++kq) r += *(const f32x4*)(pb + (size_t)kq * 32 * 65536);
  const int mt = tile >> 3, nt = tile & 7;
  const int m = mt * 128 + (w >> 1) * 64 + v * 16 + (l & 15), n = nt * 128 + (w & 1) * 64 + t * 16 + (l >> 4) * 4;
  u32x2 pkd = {pk2(r[0], r[1]), pk2(r[2], r[3])};
  *(u32x2*)(out + ((size_t)m * 2048 + br * 1024 + n) * 2) = pkd;
}

extern "C" void kernel_launch(void* const* d_in, const int* in_sizes, int n_in, void* d_out, int out_size, void* d_ws, size_t ws_size,
                              hipStream_t stream) {
  const char* xA = (const char*)d_in[0];
  const char* kA = (const char*)d_in[1];
  const char* vA = (const char*)d_in[2];
  const char* wqA = (const char*)d_in[3];
  const char* woA = (const char*)d_in[4];
  const char* xB = (const char*)d_in[5];
  const char* kB = (const char*)d_in[6];
  const char* vB = (const char*)d_in[7];
  const char* wqB = (const char*)d_in[8];
  const char* woB = (const char*)d_in[9];
  char* ows = (char*)d_ws;
  k1_qproj_attn<<<256, 512, 0, stream>>>(xA, kA, vA, wqA, xB, kB, vB, wqB, ows);
  k2_oproj<<<256, 512, 0, stream>>>(ows, woA, woB);
  k3_reduce<<<1024, 256, 0, stream>>>(ows, (char*)d_out);
}
```

```cpp
#include <hip/hip_runtime.h>
#include <hip/hip_bf16.h>

#define POL_Q 2
#define POL_KVA 2

typedef short bf16x8 __attribute__((ext_vector_type(8)));
typedef short s16x4 __attribute__((ext_vector_type(4)));
typedef float f32x4 __attribute__((ext_vector_type(4)));
typedef float f32x2 __attribute__((ext_vector_type(2)));
typedef __bf16 bf16x2 __attribute__((ext_vector_type(2)));
typedef unsigned u32x4 __attribute__((ext_vector_type(4)));
typedef unsigned u32x2 __attribute__((ext_vector_type(2)));
#define LAS __attribute__((address_space(3)))
#define DI __device__ __forceinline__

#define WAIT_V(n) asm volatile("s_waitcnt vmcnt(%0)" ::"n"(n) : "memory")
#define WAIT_L0() do { asm volatile("" ::: "memory"); __builtin_amdgcn_s_waitcnt(0xC07F); asm volatile("" ::: "memory"); } while (0)
#define BAR() __builtin_amdgcn_s_barrier()
#define SB() __builtin_amdgcn_sched_barrier(0)

template <bool NT>
DI void glds(const char* sbase, unsigned voff, unsigned ldsaddr) {
  if (NT)
    asm volatile("s_mov_b32 m0, %2\n\ts_nop 0\n\tglobal_load_lds_dwordx4 %0, %1 nt" ::"v"(voff), "s"(sbase), "s"(ldsaddr) : "memory");
  else
    asm volatile("s_mov_b32 m0, %2\n\ts_nop 0\n\tglobal_load_lds_dwordx4 %0, %1" ::"v"(voff), "s"(sbase), "s"(ldsaddr) : "memory");
}
template <bool NT>
DI void glds4(const char* sbase, unsigned v0, unsigned v1, unsigned v2, unsigned v3, unsigned ldsaddr) {
  if (NT)
    asm volatile("s_mov_b32 m0, %5\n\ts_nop 0\n\t"
                 "global_load_lds_dwordx4 %0, %4 nt\n\t"
                 "global_load_lds_dwordx4 %1, %4 offset:1024 nt\n\t"
                 "global_load_lds_dwordx4 %2, %4 offset:2048 nt\n\t"
                 "global_load_lds_dwordx4 %3, %4 offset:3072 nt"
                 ::"v"(v0), "v"(v1), "v"(v2), "v"(v3), "s"(sbase), "s"(ldsaddr) : "memory");
  else
    asm volatile("s_mov_b32 m0, %5\n\ts_nop 0\n\t"
                 "global_load_lds_dwordx4 %0, %4\n\t"
                 "global_load_lds_dwordx4 %1, %4 offset:1024\n\t"
                 "global_load_lds_dwordx4 %2, %4 offset:2048\n\t"
                 "global_load_lds_dwordx4 %3, %4 offset:3072"
                 ::"v"(v0), "v"(v1), "v"(v2), "v"(v3), "s"(sbase), "s"(ldsaddr) : "memory");
}
DI void pin(const char* sbase, unsigned voff, unsigned ldsaddr) {
  asm volatile("s_mov_b32 m0, %2\n\ts_nop 0\n\tglobal_load_lds_dword %0, %1" ::"v"(voff), "s"(sbase), "s"(ldsaddr) : "memory");
}

DI f32x4 mfma16(bf16x8 a, bf16x8 b, f32x4 c) { return __builtin_amdgcn_mfma_f32_16x16x32_bf16(a, b, c, 0, 0, 0); }
DI unsigned pk2(float lo, float hi) { f32x2 a = {lo, hi}; return __builtin_bit_cast(unsigned, __builtin_convertvector(a, bf16x2)); }
DI bf16x8 lds_b128(const char* smem, unsigned off) { return *(const bf16x8*)(smem + off); }
DI s16x4 lds_tr(char* smem, unsigned off) { return __builtin_amdgcn_ds_read_tr16_b64_v4i16((LAS s16x4*)(smem + off)); }
DI bf16x8 cat8(s16x4 lo, s16x4 hi) { return __builtin_shufflevector(lo, hi, 0, 1, 2, 3, 4, 5, 6, 7); }
DI float xg_max(float x) {
  auto s = __builtin_amdgcn_permlane16_swap(__float_as_uint(x), __float_as_uint(x), false, false);
  x = fmaxf(__uint_as_float(s[0]), __uint_as_float(s[1]));
  auto t = __builtin_amdgcn_permlane32_swap(__float_as_uint(x), __float_as_uint(x), false, false);
  return fmaxf(__uint_as_float(t[0]), __uint_as_float(t[1]));
}
DI float xg_sum(float x) {
  auto s = __builtin_amdgcn_permlane16_swap(__float_as_uint(x), __float_as_uint(x), false, false);
  x = __uint_as_float(s[0]) + __uint_as_float(s[1]);
  auto t = __builtin_amdgcn_permlane32_swap(__float_as_uint(x), __float_as_uint(x), false, false);
  return __uint_as_float(t[0]) + __uint_as_float(t[1]);
}

#define STAMP(k)
#define STAMP_DECL
#define STAMP_FLUSH(ws)
constexpr float SL2E = 0.088388347648318447f * 1.4426950408889634f;

#define ASLEEP 0

#define G8_(j, off, sfx) "global_load_lds_dwordx4 %" #j ", %8 offset:" #off sfx "\n\t"
template <int MODE>
DI void glds8(const char* sbase, const unsigned (&v)[8], unsigned ldsaddr) {
  if (MODE == 0)
    asm volatile("s_mov_b32 m0, %9\n\ts_nop 0\n\t" G8_(0, -4096, "") G8_(1, -3072, "") G8_(2, -2048, "") G8_(3, -1024, "") G8_(4, 0, "") G8_(5, 1024, "") G8_(6, 2048, "") G8_(7, 3072, "")
                 ::"v"(v[0]), "v"(v[1]), "v"(v[2]), "v"(v[3]), "v"(v[4]), "v"(v[5]), "v"(v[6]), "v"(v[7]), "s"(sbase), "s"(ldsaddr) : "memory");
  else if (MODE == 1)
    asm volatile("s_mov_b32 m0, %9\n\ts_nop 0\n\t" G8_(0, -4096, " nt") G8_(1, -3072, " nt") G8_(2, -2048, " nt") G8_(3, -1024, " nt") G8_(4, 0, " nt") G8_(5, 1024, " nt") G8_(6, 2048, " nt") G8_(7, 3072, " nt")
                 ::"v"(v[0]), "v"(v[1]), "v"(v[2]), "v"(v[3]), "v"(v[4]), "v"(v[5]), "v"(v[6]), "v"(v[7]), "s"(sbase), "s"(ldsaddr) : "memory");
  else
    asm volatile("s_mov_b32 m0, %9\n\ts_nop 0\n\t" G8_(0, -4096, " nt") G8_(0, -4096, "") G8_(1, -3072, " nt") G8_(1, -3072, "") G8_(2, -2048, " nt") G8_(2, -2048, "") G8_(3, -1024, " nt") G8_(3, -1024, "")
                 G8_(4, 0, " nt") G8_(4, 0, "") G8_(5, 1024, " nt") G8_(5, 1024, "") G8_(6, 2048, " nt") G8_(6, 2048, "") G8_(7, 3072, " nt") G8_(7, 3072, "")
                 ::"v"(v[0]), "v"(v[1]), "v"(v[2]), "v"(v[3]), "v"(v[4]), "v"(v[5]), "v"(v[6]), "v"(v[7]), "s"(sbase), "s"(ldsaddr) : "memory");
}
#define CVAR 1
#define RPG 2
#define H0SLEEP 12
#define KVAPOL 0
#define BLKBAR 0
#define BLKBAR2 0
#define BNT 1
#define FNT 1
#define FDUP 1
#define GSARG
#define GSPASS
#define GBAR() BAR()
struct HFrags { s16x4 a[4][2]; bf16x8 b[4]; };
template <int WS, int XT, bool TAIL, class TailF>
DI void gemm_core(char* smem, unsigned lbase, const char* wg, const char* xg, int rot, int w, int l, f32x4 (&acc)[4][4], TailF tail GSARG) {
  int bn_ = 1; (void)bn_;
  const int c = l & 15, g = l >> 4, q4 = c >> 2, p = l & 3;
  if (w >= 4) {
    const int iw = w & 1;
    unsigned vw[8], vx[8];
    {
      const unsigned vw0 = (unsigned)(g * WS + ((c ^ (2 * g)) * 16)), vw1 = vw0 ^ 128;
      const unsigned vx0 = XT ? (unsigned)(l * 16) : (unsigned)((l >> 3) * 2048 + (((l & 7) ^ g) * 16));
      const unsigned vx1 = XT ? vx0 : (vx0 ^ 64);
#pragma unroll
      for (int j = 0; j < 8; ++j) {
        vw[j] = ((j & 2) ? vw1 : vw0) + j * (4 * WS - 1024);
        vx[j] = XT ? vx0 + (j >> 1) * (128 * 1024 - 2048) : ((j & 1) ? vx1 : vx0) + j * (16384 - 1024);
      }
    }
    const char* w_w = wg + (size_t)(32 * iw) * WS + 4096;
    const char* x_w = (XT ? xg + (size_t)(4 * iw) * (128 * 1024) : xg + (size_t)(64 * iw) * 2048) + 4096;
    const unsigned lw0 = lbase + iw * 8192 + 4096;
    auto wpieces = [&](int kap, unsigned bufoff, bool first) {
      if (first && FNT) glds8<FDUP ? 2 : 1>(w_w + (size_t)kap * (64 * WS), vw, lw0 + bufoff);
      else glds8<0>(w_w + (size_t)kap * (64 * WS), vw, lw0 + bufoff);
    };
    auto xpieces = [&](int kap, unsigned bufoff) { glds8<0>(x_w + kap * (XT ? 2048 : 128), vx, lw0 + bufoff + 16384); };
    constexpr int FN = (FDUP && FNT) ? 24 : 16;
    if (w < 6) {
#pragma unroll 1
      for (int f = 0; f < 2; ++f) { wpieces(rot + f, f * 32768, true); xpieces(rot + f, f * 32768); }
#pragma unroll 1
      for (int i = 0; i < 2; ++i) {
        WAIT_V(FN);
        GBAR();
        GBAR();
        wpieces(rot + i + 2, i * 32768, true); xpieces(rot + i + 2, i * 32768);
        GBAR();
        GBAR();
      }
      WAIT_V(FN);
      GBAR();
      GBAR();
      if (TAIL) tail(0);
      GBAR();
      if (TAIL) tail(1);
      GBAR();
      if (TAIL) tail(2);
      WAIT_V(TAIL ? 12 : 0);
      GBAR();
      if (TAIL) tail(3);
      GBAR();
      GBAR();
      GBAR();
    } else {
      auto stage = [&](int kap, int hb) { wpieces(kap & 15, 65536 + hb * 32768, false); xpieces(kap & 15, 65536 + hb * 32768); };
      __builtin_amdgcn_s_sleep(H0SLEEP);
      stage(rot + 4, 0);
      GBAR();
      stage(rot + 8, 1);
      WAIT_V(16);
      GBAR();
      stage(rot + 12, 2);
#pragma unroll 1
      for (int i = 0; i < 3; ++i) {
        WAIT_V(16);
        GBAR();
        stage(rot + 5 + i, 0);
        WAIT_V(16);
        GBAR();
        stage(rot + 9 + i, 1);
        GBAR();
        stage(rot + 13 + i, 2);
        WAIT_V(32);
        GBAR();
      }
      if (TAIL) { tail(0); tail(1); }
      WAIT_V(TAIL ? 24 : 16);
      GBAR();
      if (TAIL) { tail(2); tail(3); }
      WAIT_V(TAIL ? 16 : 0);
      GBAR();
    }
  } else {
    const int nh = w & 1, mh = w >> 1;
    unsigned aw[4];
    {
      const int F = 2 * q4 | 8 * (g & 1);
#pragma unroll
      for (int t = 0; t < 4; ++t) aw[t] = (unsigned)((8 * g + q4) * 256 + ((((2 * (4 * nh + t)) ^ F) + (p >> 1)) * 16) + (p & 1) * 8);
    }
    const unsigned ax0 = XT ? (unsigned)(16384 + mh * 8192 + l * 16) : (unsigned)(16384 + (64 * mh + c) * 128 + ((g ^ (c >> 1)) * 16));
    const unsigned ax1 = XT ? ax0 + 1024 : (ax0 ^ 64);
    struct FF { HFrags h[2]; };
    auto mm = [&](const FF& fo) {
#pragma unroll
      for (int ks = 0; ks < 2; ++ks)
#pragma unroll
        for (int t = 0; t < 4; ++t)
#pragma unroll
          for (int v = 0; v < 4; ++v) acc[t][v] = mfma16(cat8(fo.h[ks].a[t][0], fo.h[ks].a[t][1]), fo.h[ks].b[v], acc[t][v]);
    };
    auto full = [&](FF& fn, const FF& fo, unsigned bo) {
#pragma unroll
      for (int ks = 0; ks < 2; ++ks)
#pragma unroll
        for (int t = 0; t < 4; ++t) {
          fn.h[ks].b[t] = lds_b128(smem, (ks ? ax1 : ax0) + bo + t * 2048);
          fn.h[ks].a[t][0] = lds_tr(smem, aw[t] + bo + ks * 8192);
          fn.h[ks].a[t][1] = lds_tr(smem, aw[t] + bo + ks * 8192 + 1024);
        }
      mm(fo);
#pragma unroll
      for (int t = 0; t < 24; ++t) {
        __builtin_amdgcn_sched_group_barrier(0x008, 1, 0);
        __builtin_amdgcn_sched_group_barrier(0x100, 1, 0);
      }
      __builtin_amdgcn_sched_group_barrier(0x008, 8, 0);
      SB();
    };
#pragma unroll
    for (int t = 0; t < 4; ++t)
#pragma unroll
      for (int v = 0; v < 4; ++v) acc[t][v] = f32x4{0, 0, 0, 0};
    FF f0, f1;
#pragma unroll
    for (int ks = 0; ks < 2; ++ks)
#pragma unroll
      for (int t = 0; t < 4; ++t) { f1.h[ks].b[t] = bf16x8{0, 0, 0, 0, 0, 0, 0, 0}; f1.h[ks].a[t][0] = s16x4{0, 0, 0, 0}; f1.h[ks].a[t][1] = s16x4{0, 0, 0, 0}; }
#pragma unroll 1
    for (int n = 0; n < 16; n += 2) {
      const unsigned bo0 = (n & 2) ? 98304 : ((n >> 2) & 1) * 32768, bo1 = (n & 2) ? 131072 : 65536;
      WAIT_L0();
      GBAR();
      full(f0, f1, bo0);
      WAIT_L0();
      GBAR();
      full(f1, f0, bo1);
    }
    WAIT_L0();
    mm(f1);
  }
}

__global__ __launch_bounds__(512, 2) void k1_qproj_attn(const char* __restrict__ xA, const char* __restrict__ kA, const char* __restrict__ vA,
                                                        const char* __restrict__ wqA, const char* __restrict__ xB, const char* __restrict__ kB,
                                                        const char* __restrict__ vB, const char* __restrict__ wqB, char* __restrict__ ows) {
  __shared__ __attribute__((aligned(1024))) char smem[163840];
  const unsigned lbase = (unsigned)(size_t)(LAS char*)smem;
  const int tid = threadIdx.x, l = tid & 63, w = __builtin_amdgcn_readfirstlane(tid >> 6);
  const int xcd = blockIdx.x & 7, j = blockIdx.x >> 3;
  const int br = j & 1, idx = j >> 1;
  int head, bg, kvh, hkv, rot, brot;
  if (br) { const int a = idx >> 2; bg = idx & 3; head = 4 * xcd + a; kvh = head; hkv = 32; rot = 4 * ((a + bg) & 3); brot = 0; }
  else { const int a = idx >> 1; kvh = xcd >> 1; head = 8 * kvh + a; bg = (xcd & 1) * 2 + (idx & 1); hkv = 4; rot = 4 * ((a + 2 * (idx & 1)) & 3); brot = a & 3; }
  const char* xg = (br ? xB : xA) + (size_t)bg * 128 * 2048;
  const char* wq = (br ? wqB : wqA) + head * 256;
  const int c = l & 15, g = l >> 4, q4 = c >> 2, p = l & 3;
  const int u = w & 3, dh = w >> 2;
  const unsigned ring = (unsigned)((w >= 4 ? w - 4 : w < 2 ? w + 4 : w + 6) * 16384);
  STAMP_DECL;
  STAMP(0);
  if (ASLEEP && !br) {
#pragma unroll 1
    for (int i = 0; i < ASLEEP; ++i) __builtin_amdgcn_s_sleep(16);
  }
  const int b = 4 * bg + u;
  const size_t kvoff = ((size_t)(b * hkv + kvh) * 512 + dh * 256) * 256;
  const char* kg = (br ? kB : kA) + kvoff;
  const char* vg = (br ? vB : vA) + kvoff;
  unsigned vk[4], vv[2];
#pragma unroll
  for (int i = 0; i < 4; ++i) vk[i] = (unsigned)(g * 256 + ((c ^ g ^ (4 * i)) * 16));
#pragma unroll
  for (int i = 0; i < 2; ++i) vv[i] = (unsigned)(g * 256 + ((c ^ (2 * g) ^ (8 * i)) * 16));
  auto slot_off = [](int t) constexpr { return t * 4096; };
  auto tile_issue = [&](int blk, int isV, int t) {
    const unsigned la = lbase + slot_off(t) + ring;
    const char* src = (isV ? vg : kg) + (((blk + brot) & 3) * 4 + t) * 4096;
    if (br && BNT) {
      if (isV) glds4<true>(src, vv[0], vv[1], vv[0], vv[1], la);
      else glds4<true>(src, vk[0], vk[1], vk[2], vk[3], la);
    } else {
      if (!br && KVAPOL && blk == 0 && (KVAPOL == 2 || idx < 8)) {
        if (isV) glds4<true>(src, vv[0], vv[1], vv[0], vv[1], la);
        else glds4<true>(src, vk[0], vk[1], vk[2], vk[3], la);
      }
      if (isV) glds4<false>(src, vv[0], vv[1], vv[0], vv[1], la);
      else glds4<false>(src, vk[0], vk[1], vk[2], vk[3], la);
    }
  };

  f32x4 qa[4][4];
  gemm_core<8192, 0, true>(smem, lbase, wq, xg, rot, w, l, qa, [&](int n) { tile_issue(0, 0, n); } GSPASS);
  if (w < 4) {
#pragma unroll
    for (int t = 0; t < 4; ++t)
#pragma unroll
      for (int v = 0; v < 4; ++v) {
        const int tau = 4 * (w & 1) + t;
        u32x2 pkd = {pk2(qa[t][v][0], qa[t][v][1]), pk2(qa[t][v][2], qa[t][v][3])};
        const unsigned off = 98304 + ((w >> 1) * 4 + v) * 4096 + (tau >> 1) * 1024 + ((2 * (tau & 1) + (g >> 1)) * 16 + c) * 16 + (g & 1) * 8;
        *(u32x2*)(smem + off) = pkd;
      }
  }
  WAIT_L0();
  BAR();
  bf16x8 qf[2][4];
#pragma unroll
  for (int v = 0; v < 2; ++v)
#pragma unroll
    for (int s = 0; s < 4; ++s) qf[v][s] = lds_b128(smem, 98304 + u * 8192 + v * 4096 + s * 1024 + l * 16);
  if (w < 4) {
#pragma unroll 1
    for (int t = 0; t < 4; ++t) tile_issue(0, 0, t);
  }
  STAMP(1);
#define STAMPA(k) STAMP(k)

  unsigned ak[4], av[8];
#pragma unroll
  for (int s = 0; s < 4; ++s) ak[s] = (unsigned)(ring + c * 256 + ((g ^ c ^ (4 * s)) * 16));
  {
    const int row = 4 * g + q4, F = 2 * (row & 7);
#pragma unroll
    for (int t = 0; t < 8; ++t) av[t] = (unsigned)(ring + row * 256 + ((((2 * t) ^ F) + (p >> 1)) * 16) + (p & 1) * 8);
  }
  f32x4 o[8][2];
#pragma unroll
  for (int t = 0; t < 8; ++t) { o[t][0] = f32x4{0, 0, 0, 0}; o[t][1] = f32x4{0, 0, 0, 0}; }
  float m[2] = {-__builtin_inff(), -__builtin_inff()}, ls[2] = {0.f, 0.f};
#pragma unroll 1
  for (int blk = 0; blk < 4; ++blk) {
    f32x4 sc[4][2];
#pragma unroll
    for (int t = 0; t < 4; ++t) {
      WAIT_V(12);
      bf16x8 kf[4];
#pragma unroll
      for (int s = 0; s < 4; ++s) kf[s] = lds_b128(smem, ak[s] + slot_off(t));
      WAIT_L0();
      tile_issue(blk, 1, t);
      sc[t][0] = f32x4{0, 0, 0, 0}; sc[t][1] = f32x4{0, 0, 0, 0};
#pragma unroll
      for (int s = 0; s < 4; ++s)
#pragma unroll
        for (int v = 0; v < 2; ++v) sc[t][v] = mfma16(kf[s], qf[v][s], sc[t][v]);
      SB();
    }
    bf16x8 pf[2][2];
#pragma unroll
    for (int v = 0; v < 2; ++v) {
      float mx = sc[0][v][0];
#pragma unroll
      for (int t = 0; t < 4; ++t)
#pragma unroll
        for (int i = 0; i < 4; ++i) mx = fmaxf(mx, sc[t][v][i]);
      mx = xg_max(mx) * SL2E;
      const float mn = fmaxf(m[v], mx);
      const float alpha = __builtin_amdgcn_exp2f(m[v] - mn);
      ls[v] *= alpha;
#pragma unroll
      for (int t = 0; t < 8; ++t) o[t][v] *= alpha;
      m[v] = mn;
      float sum = 0.f;
#pragma unroll
      for (int t = 0; t < 4; ++t)
#pragma unroll
        for (int i = 0; i < 4; ++i) { const float e = __builtin_amdgcn_exp2f(fmaf(sc[t][v][i], SL2E, -mn)); sc[t][v][i] = e; sum += e; }
      ls[v] += sum;
#pragma unroll
      for (int kb = 0; kb < 2; ++kb) {
        u32x4 pw = {pk2(sc[2 * kb][v][0], sc[2 * kb][v][1]), pk2(sc[2 * kb][v][2], sc[2 * kb][v][3]),
                    pk2(sc[2 * kb + 1][v][0], sc[2 * kb + 1][v][1]), pk2(sc[2 * kb + 1][v][2], sc[2 * kb + 1][v][3])};
        pf[kb][v] = __builtin_bit_cast(bf16x8, pw);
      }
    }
    const bool more = blk < 3;
    if (BLKBAR2 == 2 || (BLKBAR2 == 1 && !br)) BAR();
#pragma unroll
    for (int kb = 0; kb < 2; ++kb) {
      s16x4 vlo[8], vhi[8];
      if (kb == 0 || more) WAIT_V(12); else WAIT_V(4);
#pragma unroll
      for (int t = 0; t < 8; ++t) vlo[t] = lds_tr(smem, av[t] + slot_off(2 * kb));
      WAIT_L0();
      if (more) { tile_issue(blk + 1, 0, 2 * kb); WAIT_V(12); }
      else if (kb == 0) WAIT_V(8);
      else WAIT_V(0);
#pragma unroll
      for (int t = 0; t < 8; ++t) vhi[t] = lds_tr(smem, av[t] + slot_off(2 * kb + 1));
      WAIT_L0();
      if (more) tile_issue(blk + 1, 0, 2 * kb + 1);
#pragma unroll
      for (int t = 0; t < 8; ++t)
#pragma unroll
        for (int v = 0; v < 2; ++v) o[t][v] = mfma16(cat8(vlo[t], vhi[t]), pf[kb][v], o[t][v]);
      SB();
    }
    if (BLKBAR == 2 || (BLKBAR == 1 && !br)) BAR();
    STAMPA(2 + blk);
  }
  ls[0] = xg_sum(ls[0]);
  ls[1] = xg_sum(ls[1]);
  if (dh) {
#pragma unroll
    for (int t = 0; t < 8; ++t) { const f32x4 tmp = o[t][0]; o[t][0] = o[t][1]; o[t][1] = tmp; }
    float tm = m[0]; m[0] = m[1]; m[1] = tm;
    tm = ls[0]; ls[0] = ls[1]; ls[1] = tm;
  }
  {
#pragma unroll
    for (int t = 0; t < 8; ++t) *(f32x4*)(smem + ring + t * 1024 + l * 16) = o[t][1];
    f32x2 ml = {m[1], ls[1]};
    *(f32x2*)(smem + ring + 8192 + l * 8) = ml;
  }
  WAIT_L0();
  BAR();
  {
    const int pw = w ^ 4;
    const unsigned pring = (unsigned)((pw >= 4 ? pw - 4 : pw < 2 ? pw + 4 : pw + 6) * 16384);
    const f32x2 ml = *(const f32x2*)(smem + pring + 8192 + l * 8);
    const float mo = m[0], lo = ls[0];
    const float mm = fmaxf(mo, ml[0]);
    float a0 = __builtin_amdgcn_exp2f(mo - mm), a1 = __builtin_amdgcn_exp2f(ml[0] - mm);
    const float inv = 1.0f / (lo * a0 + ml[1] * a1);
    a0 *= inv; a1 *= inv;
    char* ob = ows + (size_t)br * (4u << 20) + ((size_t)(2 * b + dh) * 128 + head * 4) * 1024;
#pragma unroll
    for (int t = 0; t < 8; ++t) {
      const f32x4 oth = *(const f32x4*)(smem + pring + t * 1024 + l * 16);
      const f32x4 r = o[t][0] * a0 + oth * a1;
      u32x2 pkd = {pk2(r[0], r[1]), pk2(r[2], r[3])};
      *(u32x2*)(ob + (t >> 1) * 1024 + ((2 * (t & 1) + (g >> 1)) * 16 + c) * 16 + (g & 1) * 8) = pkd;
    }
  }
  STAMPA(6);
  STAMP_FLUSH(ows);
}

constexpr size_t PART_OFF = 16u << 20;
__global__ __launch_bounds__(512, 2) void k2_oproj(char* __restrict__ ows, const char* __restrict__ woA, const char* __restrict__ woB) {
  __shared__ __attribute__((aligned(1024))) char smem[163840];
  const unsigned lbase = (unsigned)(size_t)(LAS char*)smem;
  const int tid = threadIdx.x, l = tid & 63, w = __builtin_amdgcn_readfirstlane(tid >> 6);
  const int xcd = blockIdx.x & 7, j = blockIdx.x >> 3;
  const int br = xcd >> 2, kq = xcd & 3, mt = j & 3, nt = j >> 2;
  STAMP_DECL;
  STAMP(0);
  const char* og = ows + (size_t)br * (4u << 20) + ((size_t)(mt * 8) * 128 + kq * 32) * 1024;
  const char* wo = (br ? woB : woA) + (size_t)(kq * 1024) * 2048 + nt * 256;
  f32x4 acc[4][4];
  gemm_core<2048, 1, false>(smem, lbase, wo, og, 4 * ((mt + nt) & 3), w, l, acc, [](int) {} GSPASS);
  STAMP(1);
  if (w < 4) {
    char* pb = ows + PART_OFF + ((size_t)((br * 4 + kq) * 32 + mt * 8 + nt) * 4 + w) * 16384 + l * 16;
#pragma unroll
    for (int t = 0; t < 4; ++t)
#pragma unroll
      for (int v = 0; v < 4; ++v)
        *(f32x4*)(pb + (t * 4 + v) * 1024) = acc[t][v];
  }
  STAMP(2); STAMP(3); STAMP(4); STAMP(5); STAMP(6);
}
__global__ __launch_bounds__(256) void k3_reduce(const char* __restrict__ ows, char* __restrict__ out) {
  const int id = blockIdx.x * 256 + threadIdx.x;
  const int l = id & 63, v = (id >> 6) & 3, t = (id >> 8) & 3, w = (id >> 10) & 3, tile = (id >> 12) & 31, br = id >> 17;
  const char* pb = ows + PART_OFF + ((size_t)(br * 4 * 32 + tile) * 4096 + (id & 4095)) * 16;
  f32x4 r = *(const f32x4*)pb;
#pragma unroll
  for (int kq = 1; kq < 4; ++kq) r += *(const f32x4*)(pb + (size_t)kq * 32 * 65536);
  const int mt = tile >> 3, nt = tile & 7;
  const int m = mt * 128 + (w >> 1) * 64 + v * 16 + (l & 15), n = nt * 128 + (w & 1) * 64 + t * 16 + (l >> 4) * 4;
  u32x2 pkd = {pk2(r[0], r[1]), pk2(r[2], r[3])};
  *(u32x2*)(out + ((size_t)m * 2048 + br * 1024 + n) * 2) = pkd;
}

extern "C" void kernel_launch(void* const* d_in, const int* in_sizes, int n_in, void* d_out, int out_size, void* d_ws, size_t ws_size,
                              hipStream_t stream) {
  const char* xA = (const char*)d_in[0];
  const char* kA = (const char*)d_in[1];
  const char* vA = (const char*)d_in[2];
  const char* wqA = (const char*)d_in[3];
  const char* woA = (const char*)d_in[4];
  const char* xB = (const char*)d_in[5];
  const char* kB = (const char*)d_in[6];
  const char* vB = (const char*)d_in[7];
  const char* wqB = (const char*)d_in[8];
  const char* woB = (const char*)d_in[9];
  char* ows = (char*)d_ws;
  k1_qproj_attn<<<256, 512, 0, stream>>>(xA, kA, vA, wqA, xB, kB, vB, wqB, ows);
  k2_oproj<<<256, 512, 0, stream>>>(ows, woA, woB);
  k3_reduce<<<1024, 256, 0, stream>>>(ows, (char*)d_out);
}
```

```cpp
#include <hip/hip_runtime.h>
#include <hip/hip_bf16.h>

#define POL_Q 2
#define POL_KVA 2

typedef short bf16x8 __attribute__((ext_vector_type(8)));
typedef short s16x4 __attribute__((ext_vector_type(4)));
typedef float f32x4 __attribute__((ext_vector_type(4)));
typedef float f32x2 __attribute__((ext_vector_type(2)));
typedef __bf16 bf16x2 __attribute__((ext_vector_type(2)));
typedef unsigned u32x4 __attribute__((ext_vector_type(4)));
typedef unsigned u32x2 __attribute__((ext_vector_type(2)));
#define LAS __attribute__((address_space(3)))
#define DI __device__ __forceinline__

#define WAIT_V(n) asm volatile("s_waitcnt vmcnt(%0)" ::"n"(n) : "memory")
#define WAIT_L0() do { asm volatile("" ::: "memory"); __builtin_amdgcn_s_waitcnt(0xC07F); asm volatile("" ::: "memory"); } while (0)
#define BAR() __builtin_amdgcn_s_barrier()
#define SB() __builtin_amdgcn_sched_barrier(0)

template <bool NT>
DI void glds(const char* sbase, unsigned voff, unsigned ldsaddr) {
  if (NT)
    asm volatile("s_mov_b32 m0, %2\n\ts_nop 0\n\tglobal_load_lds_dwordx4 %0, %1 nt" ::"v"(voff), "s"(sbase), "s"(ldsaddr) : "memory");
  else
    asm volatile("s_mov_b32 m0, %2\n\ts_nop 0\n\tglobal_load_lds_dwordx4 %0, %1" ::"v"(voff), "s"(sbase), "s"(ldsaddr) : "memory");
}
template <bool NT>
DI void glds4(const char* sbase, unsigned v0, unsigned v1, unsigned v2, unsigned v3, unsigned ldsaddr) {
  if (NT)
    asm volatile("s_mov_b32 m0, %5\n\ts_nop 0\n\t"
                 "global_load_lds_dwordx4 %0, %4 nt\n\t"
                 "global_load_lds_dwordx4 %1, %4 offset:1024 nt\n\t"
                 "global_load_lds_dwordx4 %2, %4 offset:2048 nt\n\t"
                 "global_load_lds_dwordx4 %3, %4 offset:3072 nt"
                 ::"v"(v0), "v"(v1), "v"(v2), "v"(v3), "s"(sbase), "s"(ldsaddr) : "memory");
  else
    asm volatile("s_mov_b32 m0, %5\n\ts_nop 0\n\t"
                 "global_load_lds_dwordx4 %0, %4\n\t"
                 "global_load_lds_dwordx4 %1, %4 offset:1024\n\t"
                 "global_load_lds_dwordx4 %2, %4 offset:2048\n\t"
                 "global_load_lds_dwordx4 %3, %4 offset:3072"
                 ::"v"(v0), "v"(v1), "v"(v2), "v"(v3), "s"(sbase), "s"(ldsaddr) : "memory");
}
DI void pin(const char* sbase, unsigned voff, unsigned ldsaddr) {
  asm volatile("s_mov_b32 m0, %2\n\ts_nop 0\n\tglobal_load_lds_dword %0, %1" ::"v"(voff), "s"(sbase), "s"(ldsaddr) : "memory");
}

DI f32x4 mfma16(bf16x8 a, bf16x8 b, f32x4 c) { return __builtin_amdgcn_mfma_f32_16x16x32_bf16(a, b, c, 0, 0, 0); }
DI unsigned pk2(float lo, float hi) { f32x2 a = {lo, hi}; return __builtin_bit_cast(unsigned, __builtin_convertvector(a, bf16x2)); }
DI bf16x8 lds_b128(const char* smem, unsigned off) { return *(const bf16x8*)(smem + off); }
DI s16x4 lds_tr(char* smem, unsigned off) { return __builtin_amdgcn_ds_read_tr16_b64_v4i16((LAS s16x4*)(smem + off)); }
DI bf16x8 cat8(s16x4 lo, s16x4 hi) { return __builtin_shufflevector(lo, hi, 0, 1, 2, 3, 4, 5, 6, 7); }
DI float xg_max(float x) {
  auto s = __builtin_amdgcn_permlane16_swap(__float_as_uint(x), __float_as_uint(x), false, false);
  x = fmaxf(__uint_as_float(s[0]), __uint_as_float(s[1]));
  auto t = __builtin_amdgcn_permlane32_swap(__float_as_uint(x), __float_as_uint(x), false, false);
  return fmaxf(__uint_as_float(t[0]), __uint_as_float(t[1]));
}
DI float xg_sum(float x) {
  auto s = __builtin_amdgcn_permlane16_swap(__float_as_uint(x), __float_as_uint(x), false, false);
  x = __uint_as_float(s[0]) + __uint_as_float(s[1]);
  auto t = __builtin_amdgcn_permlane32_swap(__float_as_uint(x), __float_as_uint(x), false, false);
  return __uint_as_float(t[0]) + __uint_as_float(t[1]);
}

#define STAMP(k)
#define STAMP_DECL
#define STAMP_FLUSH(ws)
constexpr float SL2E = 0.088388347648318447f * 1.4426950408889634f;

constexpr size_t PART_OFF = 16u << 20, CNT_OFF = 8u << 20;
#define ASLEEP 0

#define G8_(j, off, sfx) "global_load_lds_dwordx4 %" #j ", %8 offset:" #off sfx "\n\t"
template <int MODE>
DI void glds8(const char* sbase, const unsigned (&v)[8], unsigned ldsaddr) {
  if (MODE == 0)
    asm volatile("s_mov_b32 m0, %9\n\ts_nop 0\n\t" G8_(0, -4096, "") G8_(1, -3072, "") G8_(2, -2048, "") G8_(3, -1024, "") G8_(4, 0, "") G8_(5, 1024, "") G8_(6, 2048, "") G8_(7, 3072, "")
                 ::"v"(v[0]), "v"(v[1]), "v"(v[2]), "v"(v[3]), "v"(v[4]), "v"(v[5]), "v"(v[6]), "v"(v[7]), "s"(sbase), "s"(ldsaddr) : "memory");
  else if (MODE == 1)
    asm volatile("s_mov_b32 m0, %9\n\ts_nop 0\n\t" G8_(0, -4096, " nt") G8_(1, -3072, " nt") G8_(2, -2048, " nt") G8_(3, -1024, " nt") G8_(4, 0, " nt") G8_(5, 1024, " nt") G8_(6, 2048, " nt") G8_(7, 3072, " nt")
                 ::"v"(v[0]), "v"(v[1]), "v"(v[2]), "v"(v[3]), "v"(v[4]), "v"(v[5]), "v"(v[6]), "v"(v[7]), "s"(sbase), "s"(ldsaddr) : "memory");
  else
    asm volatile("s_mov_b32 m0, %9\n\ts_nop 0\n\t" G8_(0, -4096, " nt") G8_(0, -4096, "") G8_(1, -3072, " nt") G8_(1, -3072, "") G8_(2, -2048, " nt") G8_(2, -2048, "") G8_(3, -1024, " nt") G8_(3, -1024, "")
                 G8_(4, 0, " nt") G8_(4, 0, "") G8_(5, 1024, " nt") G8_(5, 1024, "") G8_(6, 2048, " nt") G8_(6, 2048, "") G8_(7, 3072, " nt") G8_(7, 3072, "")
                 ::"v"(v[0]), "v"(v[1]), "v"(v[2]), "v"(v[3]), "v"(v[4]), "v"(v[5]), "v"(v[6]), "v"(v[7]), "s"(sbase), "s"(ldsaddr) : "memory");
}
#define CVAR 1
#define RPG 2
#define H0SLEEP 12
#define KVAPOL 0
#define BLKBAR 0
#define BLKBAR2 0
#define BNT 1
#define FNT 1
#define FDUP 1
#define GSARG
#define GSPASS
#define GBAR() BAR()
struct HFrags { s16x4 a[4][2]; bf16x8 b[4]; };
template <int WS, int XT, bool TAIL, class TailF>
DI void gemm_core(char* smem, unsigned lbase, const char* wg, const char* xg, int rot, int w, int l, f32x4 (&acc)[4][4], TailF tail GSARG) {
  int bn_ = 1; (void)bn_;
  const int c = l & 15, g = l >> 4, q4 = c >> 2, p = l & 3;
  if (w >= 4) {
    const int iw = w & 1;
    unsigned vw[8], vx[8];
    {
      const unsigned vw0 = (unsigned)(g * WS + ((c ^ (2 * g)) * 16)), vw1 = vw0 ^ 128;
      const unsigned vx0 = XT ? (unsigned)(l * 16) : (unsigned)((l >> 3) * 2048 + (((l & 7) ^ g) * 16));
      const unsigned vx1 = XT ? vx0 : (vx0 ^ 64);
#pragma unroll
      for (int j = 0; j < 8; ++j) {
        vw[j] = ((j & 2) ? vw1 : vw0) + j * (4 * WS - 1024);
        vx[j] = XT ? vx0 + (j >> 1) * (128 * 1024 - 2048) : ((j & 1) ? vx1 : vx0) + j * (16384 - 1024);
      }
    }
    const char* w_w = wg + (size_t)(32 * iw) * WS + 4096;
    const char* x_w = (XT ? xg + (size_t)(4 * iw) * (128 * 1024) : xg + (size_t)(64 * iw) * 2048) + 4096;
    const unsigned lw0 = lbase + iw * 8192 + 4096;
    auto wpieces = [&](int kap, unsigned bufoff, bool first) {
      if (first && FNT) glds8<FDUP ? 2 : 1>(w_w + (size_t)kap * (64 * WS), vw, lw0 + bufoff);
      else glds8<0>(w_w + (size_t)kap * (64 * WS), vw, lw0 + bufoff);
    };
    auto xpieces = [&](int kap, unsigned bufoff) { glds8<0>(x_w + kap * (XT ? 2048 : 128), vx, lw0 + bufoff + 16384); };
    constexpr int FN = (FDUP && FNT) ? 24 : 16;
    if (w < 6) {
#pragma unroll 1
      for (int f = 0; f < 2; ++f) { wpieces(rot + f, f * 32768, true); xpieces(rot + f, f * 32768); }
#pragma unroll 1
      for (int i = 0; i < 2; ++i) {
        WAIT_V(FN);
        GBAR();
        GBAR();
        wpieces(rot + i + 2, i * 32768, true); xpieces(rot + i + 2, i * 32768);
        GBAR();
        GBAR();
      }
      WAIT_V(FN);
      GBAR();
      GBAR();
      if (TAIL) tail(0);
      GBAR();
      if (TAIL) tail(1);
      GBAR();
      if (TAIL) tail(2);
      WAIT_V(TAIL ? 12 : 0);
      GBAR();
      if (TAIL) tail(3);
      GBAR();
      GBAR();
      GBAR();
    } else {
      auto stage = [&](int kap, int hb) { wpieces(kap & 15, 65536 + hb * 32768, false); xpieces(kap & 15, 65536 + hb * 32768); };
      __builtin_amdgcn_s_sleep(H0SLEEP);
      stage(rot + 4, 0);
      GBAR();
      stage(rot + 8, 1);
      WAIT_V(16);
      GBAR();
      stage(rot + 12, 2);
#pragma unroll 1
      for (int i = 0; i < 3; ++i) {
        WAIT_V(16);
        GBAR();
        stage(rot + 5 + i, 0);
        WAIT_V(16);
        GBAR();
        stage(rot + 9 + i, 1);
        GBAR();
        stage(rot + 13 + i, 2);
        WAIT_V(32);
        GBAR();
      }
      if (TAIL) { tail(0); tail(1); }
      WAIT_V(TAIL ? 24 : 16);
      GBAR();
      if (TAIL) { tail(2); tail(3); }
      WAIT_V(TAIL ? 16 : 0);
      GBAR();
    }
  } else {
    const int nh = w & 1, mh = w >> 1;
    unsigned aw[4];
    {
      const int F = 2 * q4 | 8 * (g & 1);
#pragma unroll
      for (int t = 0; t < 4; ++t) aw[t] = (unsigned)((8 * g + q4) * 256 + ((((2 * (4 * nh + t)) ^ F) + (p >> 1)) * 16) + (p & 1) * 8);
    }
    const unsigned ax0 = XT ? (unsigned)(16384 + mh * 8192 + l * 16) : (unsigned)(16384 + (64 * mh + c) * 128 + ((g ^ (c >> 1)) * 16));
    const unsigned ax1 = XT ? ax0 + 1024 : (ax0 ^ 64);
    struct FF { HFrags h[2]; };
    auto mm = [&](const FF& fo) {
#pragma unroll
      for (int ks = 0; ks < 2; ++ks)
#pragma unroll
        for (int t = 0; t < 4; ++t)
#pragma unroll
          for (int v = 0; v < 4; ++v) acc[t][v] = mfma16(cat8(fo.h[ks].a[t][0], fo.h[ks].a[t][1]), fo.h[ks].b[v], acc[t][v]);
    };
    auto full = [&](FF& fn, const FF& fo, unsigned bo) {
#pragma unroll
      for (int ks = 0; ks < 2; ++ks)
#pragma unroll
        for (int t = 0; t < 4; ++t) {
          fn.h[ks].b[t] = lds_b128(smem, (ks ? ax1 : ax0) + bo + t * 2048);
          fn.h[ks].a[t][0] = lds_tr(smem, aw[t] + bo + ks * 8192);
          fn.h[ks].a[t][1] = lds_tr(smem, aw[t] + bo + ks * 8192 + 1024);
        }
      mm(fo);
#pragma unroll
      for (int t = 0; t < 24; ++t) {
        __builtin_amdgcn_sched_group_barrier(0x008, 1, 0);
        __builtin_amdgcn_sched_group_barrier(0x100, 1, 0);
      }
      __builtin_amdgcn_sched_group_barrier(0x008, 8, 0);
      SB();
    };
#pragma unroll
    for (int t = 0; t < 4; ++t)
#pragma unroll
      for (int v = 0; v < 4; ++v) acc[t][v] = f32x4{0, 0, 0, 0};
    FF f0, f1;
#pragma unroll
    for (int ks = 0; ks < 2; ++ks)
#pragma unroll
      for (int t = 0; t < 4; ++t) { f1.h[ks].b[t] = bf16x8{0, 0, 0, 0, 0, 0, 0, 0}; f1.h[ks].a[t][0] = s16x4{0, 0, 0, 0}; f1.h[ks].a[t][1] = s16x4{0, 0, 0, 0}; }
#pragma unroll 1
    for (int n = 0; n < 16; n += 2) {
      const unsigned bo0 = (n & 2) ? 98304 : ((n >> 2) & 1) * 32768, bo1 = (n & 2) ? 131072 : 65536;
      WAIT_L0();
      GBAR();
      full(f0, f1, bo0);
      WAIT_L0();
      GBAR();
      full(f1, f0, bo1);
    }
    WAIT_L0();
    mm(f1);
  }
}

__global__ __launch_bounds__(512, 2) void k1_qproj_attn(const char* __restrict__ xA, const char* __restrict__ kA, const char* __restrict__ vA,
                                                        const char* __restrict__ wqA, const char* __restrict__ xB, const char* __restrict__ kB,
                                                        const char* __restrict__ vB, const char* __restrict__ wqB, char* __restrict__ ows) {
  __shared__ __attribute__((aligned(1024))) char smem[163840];
  const unsigned lbase = (unsigned)(size_t)(LAS char*)smem;
  const int tid = threadIdx.x, l = tid & 63, w = __builtin_amdgcn_readfirstlane(tid >> 6);
  const int xcd = blockIdx.x & 7, j = blockIdx.x >> 3;
  const int br = j & 1, idx = j >> 1;
  int head, bg, kvh, hkv, rot, brot;
  if (br) { const int a = idx >> 2; bg = idx & 3; head = 4 * xcd + a; kvh = head; hkv = 32; rot = 4 * ((a + bg) & 3); brot = 0; }
  else { const int a = idx >> 1; kvh = xcd >> 1; head = 8 * kvh + a; bg = (xcd & 1) * 2 + (idx & 1); hkv = 4; rot = 4 * ((a + 2 * (idx & 1)) & 3); brot = a & 3; }
  const char* xg = (br ? xB : xA) + (size_t)bg * 128 * 2048;
  const char* wq = (br ? wqB : wqA) + head * 256;
  const int c = l & 15, g = l >> 4, q4 = c >> 2, p = l & 3;
  const int u = w & 3, dh = w >> 2;
  const unsigned ring = (unsigned)((w >= 4 ? w - 4 : w < 2 ? w + 4 : w + 6) * 16384);
  STAMP_DECL;
  STAMP(0);
  if (ASLEEP && !br) {
#pragma unroll 1
    for (int i = 0; i < ASLEEP; ++i) __builtin_amdgcn_s_sleep(16);
  }
  const int b = 4 * bg + u;
  const size_t kvoff = ((size_t)(b * hkv + kvh) * 512 + dh * 256) * 256;
  const char* kg = (br ? kB : kA) + kvoff;
  const char* vg = (br ? vB : vA) + kvoff;
  unsigned vk[4], vv[2];
#pragma unroll
  for (int i = 0; i < 4; ++i) vk[i] = (unsigned)(g * 256 + ((c ^ g ^ (4 * i)) * 16));
#pragma unroll
  for (int i = 0; i < 2; ++i) vv[i] = (unsigned)(g * 256 + ((c ^ (2 * g) ^ (8 * i)) * 16));
  auto slot_off = [](int t) constexpr { return t * 4096; };
  auto tile_issue = [&](int blk, int isV, int t) {
    const unsigned la = lbase + slot_off(t) + ring;
    const char* src = (isV ? vg : kg) + (((blk + brot) & 3) * 4 + t) * 4096;
    if (br && BNT) {
      if (isV) glds4<true>(src, vv[0], vv[1], vv[0], vv[1], la);
      else glds4<true>(src, vk[0], vk[1], vk[2], vk[3], la);
    } else {
      if (!br && KVAPOL && blk == 0 && (KVAPOL == 2 || idx < 8)) {
        if (isV) glds4<true>(src, vv[0], vv[1], vv[0], vv[1], la);
        else glds4<true>(src, vk[0], vk[1], vk[2], vk[3], la);
      }
      if (isV) glds4<false>(src, vv[0], vv[1], vv[0], vv[1], la);
      else glds4<false>(src, vk[0], vk[1], vk[2], vk[3], la);
    }
  };

  f32x4 qa[4][4];
  gemm_core<8192, 0, true>(smem, lbase, wq, xg, rot, w, l, qa, [&](int n) { tile_issue(0, 0, n); } GSPASS);
  if (w < 4) {
#pragma unroll
    for (int t = 0; t < 4; ++t)
#pragma unroll
      for (int v = 0; v < 4; ++v) {
        const int tau = 4 * (w & 1) + t;
        u32x2 pkd = {pk2(qa[t][v][0], qa[t][v][1]), pk2(qa[t][v][2], qa[t][v][3])};
        const unsigned off = 98304 + ((w >> 1) * 4 + v) * 4096 + (tau >> 1) * 1024 + ((2 * (tau & 1) + (g >> 1)) * 16 + c) * 16 + (g & 1) * 8;
        *(u32x2*)(smem + off) = pkd;
      }
  }
  WAIT_L0();
  BAR();
  bf16x8 qf[2][4];
#pragma unroll
  for (int v = 0; v < 2; ++v)
#pragma unroll
    for (int s = 0; s < 4; ++s) qf[v][s] = lds_b128(smem, 98304 + u * 8192 + v * 4096 + s * 1024 + l * 16);
  if (w < 4) {
#pragma unroll 1
    for (int t = 0; t < 4; ++t) tile_issue(0, 0, t);
  }
  STAMP(1);
#define STAMPA(k) STAMP(k)

  unsigned ak[4], av[8];
#pragma unroll
  for (int s = 0; s < 4; ++s) ak[s] = (unsigned)(ring + c * 256 + ((g ^ c ^ (4 * s)) * 16));
  {
    const int row = 4 * g + q4, F = 2 * (row & 7);
#pragma unroll
    for (int t = 0; t < 8; ++t) av[t] = (unsigned)(ring + row * 256 + ((((2 * t) ^ F) + (p >> 1)) * 16) + (p & 1) * 8);
  }
  f32x4 o[8][2];
#pragma unroll
  for (int t = 0; t < 8; ++t) { o[t][0] = f32x4{0, 0, 0, 0}; o[t][1] = f32x4{0, 0, 0, 0}; }
  float m[2] = {-__builtin_inff(), -__builtin_inff()}, ls[2] = {0.f, 0.f};
#pragma unroll 1
  for (int blk = 0; blk < 4; ++blk) {
    f32x4 sc[4][2];
#pragma unroll
    for (int t = 0; t < 4; ++t) {
      WAIT_V(12);
      bf16x8 kf[4];
#pragma unroll
      for (int s = 0; s < 4; ++s) kf[s] = lds_b128(smem, ak[s] + slot_off(t));
      WAIT_L0();
      tile_issue(blk, 1, t);
      sc[t][0] = f32x4{0, 0, 0, 0}; sc[t][1] = f32x4{0, 0, 0, 0};
#pragma unroll
      for (int s = 0; s < 4; ++s)
#pragma unroll
        for (int v = 0; v < 2; ++v) sc[t][v] = mfma16(kf[s], qf[v][s], sc[t][v]);
      SB();
    }
    bf16x8 pf[2][2];
#pragma unroll
    for (int v = 0; v < 2; ++v) {
      float mx = sc[0][v][0];
#pragma unroll
      for (int t = 0; t < 4; ++t)
#pragma unroll
        for (int i = 0; i < 4; ++i) mx = fmaxf(mx, sc[t][v][i]);
      mx = xg_max(mx) * SL2E;
      const float mn = fmaxf(m[v], mx);
      const float alpha = __builtin_amdgcn_exp2f(m[v] - mn);
      ls[v] *= alpha;
#pragma unroll
      for (int t = 0; t < 8; ++t) o[t][v] *= alpha;
      m[v] = mn;
      float sum = 0.f;
#pragma unroll
      for (int t = 0; t < 4; ++t)
#pragma unroll
        for (int i = 0; i < 4; ++i) { const float e = __builtin_amdgcn_exp2f(fmaf(sc[t][v][i], SL2E, -mn)); sc[t][v][i] = e; sum += e; }
      ls[v] += sum;
#pragma unroll
      for (int kb = 0; kb < 2; ++kb) {
        u32x4 pw = {pk2(sc[2 * kb][v][0], sc[2 * kb][v][1]), pk2(sc[2 * kb][v][2], sc[2 * kb][v][3]),
                    pk2(sc[2 * kb + 1][v][0], sc[2 * kb + 1][v][1]), pk2(sc[2 * kb + 1][v][2], sc[2 * kb + 1][v][3])};
        pf[kb][v] = __builtin_bit_cast(bf16x8, pw);
      }
    }
    const bool more = blk < 3;
    if (BLKBAR2 == 2 || (BLKBAR2 == 1 && !br)) BAR();
#pragma unroll
    for (int kb = 0; kb < 2; ++kb) {
      s16x4 vlo[8], vhi[8];
      if (kb == 0 || more) WAIT_V(12); else WAIT_V(4);
#pragma unroll
      for (int t = 0; t < 8; ++t) vlo[t] = lds_tr(smem, av[t] + slot_off(2 * kb));
      WAIT_L0();
      if (more) { tile_issue(blk + 1, 0, 2 * kb); WAIT_V(12); }
      else if (kb == 0) WAIT_V(8);
      else WAIT_V(0);
#pragma unroll
      for (int t = 0; t < 8; ++t) vhi[t] = lds_tr(smem, av[t] + slot_off(2 * kb + 1));
      WAIT_L0();
      if (more) tile_issue(blk + 1, 0, 2 * kb + 1);
#pragma unroll
      for (int t = 0; t < 8; ++t)
#pragma unroll
        for (int v = 0; v < 2; ++v) o[t][v] = mfma16(cat8(vlo[t], vhi[t]), pf[kb][v], o[t][v]);
      SB();
    }
    if (BLKBAR == 2 || (BLKBAR == 1 && !br)) BAR();
    STAMPA(2 + blk);
  }
  ls[0] = xg_sum(ls[0]);
  ls[1] = xg_sum(ls[1]);
  if (dh) {
#pragma unroll
    for (int t = 0; t < 8; ++t) { const f32x4 tmp = o[t][0]; o[t][0] = o[t][1]; o[t][1] = tmp; }
    float tm = m[0]; m[0] = m[1]; m[1] = tm;
    tm = ls[0]; ls[0] = ls[1]; ls[1] = tm;
  }
  {
#pragma unroll
    for (int t = 0; t < 8; ++t) *(f32x4*)(smem + ring + t * 1024 + l * 16) = o[t][1];
    f32x2 ml = {m[1], ls[1]};
    *(f32x2*)(smem + ring + 8192 + l * 8) = ml;
  }
  WAIT_L0();
  BAR();
  {
    const int pw = w ^ 4;
    const unsigned pring = (unsigned)((pw >= 4 ? pw - 4 : pw < 2 ? pw + 4 : pw + 6) * 16384);
    const f32x2 ml = *(const f32x2*)(smem + pring + 8192 + l * 8);
    const float mo = m[0], lo = ls[0];
    const float mm = fmaxf(mo, ml[0]);
    float a0 = __builtin_amdgcn_exp2f(mo - mm), a1 = __builtin_amdgcn_exp2f(ml[0] - mm);
    const float inv = 1.0f / (lo * a0 + ml[1] * a1);
    a0 *= inv; a1 *= inv;
    char* ob = ows + (size_t)br * (4u << 20) + ((size_t)(2 * b + dh) * 128 + head * 4) * 1024;
#pragma unroll
    for (int t = 0; t < 8; ++t) {
      const f32x4 oth = *(const f32x4*)(smem + pring + t * 1024 + l * 16);
      const f32x4 r = o[t][0] * a0 + oth * a1;
      u32x2 pkd = {pk2(r[0], r[1]), pk2(r[2], r[3])};
      *(u32x2*)(ob + (t >> 1) * 1024 + ((2 * (t & 1) + (g >> 1)) * 16 + c) * 16 + (g & 1) * 8) = pkd;
    }
  }
  if (tid == 0) *(unsigned*)(ows + CNT_OFF + blockIdx.x * 128) = 0u;
  STAMPA(6);
  STAMP_FLUSH(ows);
}

__global__ __launch_bounds__(512, 2) void k2_oproj(char* __restrict__ ows, const char* __restrict__ woA, const char* __restrict__ woB, char* __restrict__ out) {
  __shared__ __attribute__((aligned(1024))) char smem[163840];
  const unsigned lbase = (unsigned)(size_t)(LAS char*)smem;
  const int tid = threadIdx.x, l = tid & 63, w = __builtin_amdgcn_readfirstlane(tid >> 6);
  const int xcd = blockIdx.x & 7, j = blockIdx.x >> 3;
  const int br = xcd >> 2, kq = xcd & 3, mt = j & 3, nt = j >> 2;
  STAMP_DECL;
  STAMP(0);
  const char* og = ows + (size_t)br * (4u << 20) + ((size_t)(mt * 8) * 128 + kq * 32) * 1024;
  const char* wo = (br ? woB : woA) + (size_t)(kq * 1024) * 2048 + nt * 256;
  f32x4 acc[4][4];
  gemm_core<2048, 1, false>(smem, lbase, wo, og, 4 * ((mt + nt) & 3), w, l, acc, [](int) {} GSPASS);
  STAMP(1);
  const int tile = br * 32 + j;
  const __amdgpu_buffer_rsrc_t rs = __builtin_amdgcn_make_buffer_rsrc(ows + PART_OFF + (size_t)tile * (16 * 16384), 0, 16 * 16384, 0x00020000);
  unsigned* cnt = (unsigned*)(ows + CNT_OFF) + tile * 4 * 32;
  if (w < 4) {
    if (w != kq) {
      const int off = (w * 4 + kq) * 16384 + l * 16;
#pragma unroll
      for (int t = 0; t < 4; ++t)
#pragma unroll
        for (int v = 0; v < 4; ++v) __builtin_amdgcn_raw_buffer_store_b128(__builtin_bit_cast(u32x4, acc[t][v]), rs, off + (t * 4 + v) * 1024, 0, 16);
      WAIT_V(0);
      if (l == 0) __hip_atomic_fetch_add(cnt + w * 32, 1u, __ATOMIC_RELAXED, __HIP_MEMORY_SCOPE_AGENT);
    } else {
#pragma unroll
      for (int t = 0; t < 4; ++t)
#pragma unroll
        for (int v = 0; v < 4; ++v) *(f32x4*)(smem + (t * 4 + v) * 1024 + l * 16) = acc[t][v];
      WAIT_L0();
    }
  } else if (w == 4) {
    unsigned spins = 0;
    while (__hip_atomic_load(cnt + kq * 32, __ATOMIC_RELAXED, __HIP_MEMORY_SCOPE_AGENT) < 3u && ++spins < (1u << 22)) __builtin_amdgcn_s_sleep(1);
  }
  STAMP(2);
  asm volatile("" ::: "memory");
  BAR();
  asm volatile("" ::: "memory");
  STAMP(3);
  {
    u32x4 pr[3][2];
#pragma unroll
    for (int s = 0; s < 3; ++s) {
      const int src = s + (s >= kq);
#pragma unroll
      for (int i = 0; i < 2; ++i) pr[s][i] = __builtin_amdgcn_raw_buffer_load_b128(rs, (kq * 4 + src) * 16384 + (2 * w + i) * 1024 + l * 16, 0, 16);
    }
    const int c = l & 15, g = l >> 4;
#pragma unroll
    for (int i = 0; i < 2; ++i) {
      f32x4 p[4];
      const f32x4 own = *(const f32x4*)(smem + (2 * w + i) * 1024 + l * 16);
      f32x4 r = (__builtin_bit_cast(f32x4, pr[0][i]) + __builtin_bit_cast(f32x4, pr[1][i])) + (__builtin_bit_cast(f32x4, pr[2][i]) + own);
      (void)p;
      const int m = mt * 128 + (kq >> 1) * 64 + ((w & 1) * 2 + i) * 16 + c, n = nt * 128 + (kq & 1) * 64 + (w >> 1) * 16 + g * 4;
      u32x2 pkd = {pk2(r[0], r[1]), pk2(r[2], r[3])};
      *(u32x2*)(out + ((size_t)m * 2048 + br * 1024 + n) * 2) = pkd;
    }
  }
  STAMP(4); STAMP(5); STAMP(6);
}

__global__ void k_tiny(unsigned* p) { p[threadIdx.x * 32] = 0u; }
extern "C" void kernel_launch(void* const* d_in, const int* in_sizes, int n_in, void* d_out, int out_size, void* d_ws, size_t ws_size,
                              hipStream_t stream) {
  const char* xA = (const char*)d_in[0];
  const char* kA = (const char*)d_in[1];
  const char* vA = (const char*)d_in[2];
  const char* wqA = (const char*)d_in[3];
  const char* woA = (const char*)d_in[4];
  const char* xB = (const char*)d_in[5];
  const char* kB = (const char*)d_in[6];
  const char* vB = (const char*)d_in[7];
  const char* wqB = (const char*)d_in[8];
  const char* woB = (const char*)d_in[9];
  char* ows = (char*)d_ws;
  k1_qproj_attn<<<256, 512, 0, stream>>>(xA, kA, vA, wqA, xB, kB, vB, wqB, ows);
  k2_oproj<<<256, 512, 0, stream>>>(ows, woA, woB, (char*)d_out);
}
```

```cpp
#include <hip/hip_runtime.h>
#include <hip/hip_bf16.h>

#define POL_Q 2
#define POL_KVA 2

typedef short bf16x8 __attribute__((ext_vector_type(8)));
typedef short s16x4 __attribute__((ext_vector_type(4)));
typedef float f32x4 __attribute__((ext_vector_type(4)));
typedef float f32x2 __attribute__((ext_vector_type(2)));
typedef __bf16 bf16x2 __attribute__((ext_vector_type(2)));
typedef unsigned u32x4 __attribute__((ext_vector_type(4)));
typedef unsigned u32x2 __attribute__((ext_vector_type(2)));
#define LAS __attribute__((address_space(3)))
#define DI __device__ __forceinline__

#define WAIT_V(n) asm volatile("s_waitcnt vmcnt(%0)" ::"n"(n) : "memory")
#define WAIT_L0() do { asm volatile("" ::: "memory"); __builtin_amdgcn_s_waitcnt(0xC07F); asm volatile("" ::: "memory"); } while (0)
#define BAR() __builtin_amdgcn_s_barrier()
#define SB() __builtin_amdgcn_sched_barrier(0)

template <bool NT>
DI void glds(const char* sbase, unsigned voff, unsigned ldsaddr) {
  if (NT)
    asm volatile("s_mov_b32 m0, %2\n\ts_nop 0\n\tglobal_load_lds_dwordx4 %0, %1 nt" ::"v"(voff), "s"(sbase), "s"(ldsaddr) : "memory");
  else
    asm volatile("s_mov_b32 m0, %2\n\ts_nop 0\n\tglobal_load_lds_dwordx4 %0, %1" ::"v"(voff), "s"(sbase), "s"(ldsaddr) : "memory");
}
template <bool NT>
DI void glds4(const char* sbase, unsigned v0, unsigned v1, unsigned v2, unsigned v3, unsigned ldsaddr) {
  if (NT)
    asm volatile("s_mov_b32 m0, %5\n\ts_nop 0\n\t"
                 "global_load_lds_dwordx4 %0, %4 nt\n\t"
                 "global_load_lds_dwordx4 %1, %4 offset:1024 nt\n\t"
                 "global_load_lds_dwordx4 %2, %4 offset:2048 nt\n\t"
                 "global_load_lds_dwordx4 %3, %4 offset:3072 nt"
                 ::"v"(v0), "v"(v1), "v"(v2), "v"(v3), "s"(sbase), "s"(ldsaddr) : "memory");
  else
    asm volatile("s_mov_b32 m0, %5\n\ts_nop 0\n\t"
                 "global_load_lds_dwordx4 %0, %4\n\t"
                 "global_load_lds_dwordx4 %1, %4 offset:1024\n\t"
                 "global_load_lds_dwordx4 %2, %4 offset:2048\n\t"
                 "global_load_lds_dwordx4 %3, %4 offset:3072"
                 ::"v"(v0), "v"(v1), "v"(v2), "v"(v3), "s"(sbase), "s"(ldsaddr) : "memory");
}
DI void pin(const char* sbase, unsigned voff, unsigned ldsaddr) {
  asm volatile("s_mov_b32 m0, %2\n\ts_nop 0\n\tglobal_load_lds_dword %0, %1" ::"v"(voff), "s"(sbase), "s"(ldsaddr) : "memory");
}

DI f32x4 mfma16(bf16x8 a, bf16x8 b, f32x4 c) { return __builtin_amdgcn_mfma_f32_16x16x32_bf16(a, b, c, 0, 0, 0); }
DI unsigned pk2(float lo, float hi) { f32x2 a = {lo, hi}; return __builtin_bit_cast(unsigned, __builtin_convertvector(a, bf16x2)); }
DI bf16x8 lds_b128(const char* smem, unsigned off) { return *(const bf16x8*)(smem + off); }
DI s16x4 lds_tr(char* smem, unsigned off) { return __builtin_amdgcn_ds_read_tr16_b64_v4i16((LAS s16x4*)(smem + off)); }
DI bf16x8 cat8(s16x4 lo, s16x4 hi) { return __builtin_shufflevector(lo, hi, 0, 1, 2, 3, 4, 5, 6, 7); }
DI float xg_max(float x) {
  auto s = __builtin_amdgcn_permlane16_swap(__float_as_uint(x), __float_as_uint(x), false, false);
  x = fmaxf(__uint_as_float(s[0]), __uint_as_float(s[1]));
  auto t = __builtin_amdgcn_permlane32_swap(__float_as_uint(x), __float_as_uint(x), false, false);
  return fmaxf(__uint_as_float(t[0]), __uint_as_float(t[1]));
}
DI float xg_sum(float x) {
  auto s = __builtin_amdgcn_permlane16_swap(__float_as_uint(x), __float_as_uint(x), false, false);
  x = __uint_as_float(s[0]) + __uint_as_float(s[1]);
  auto t = __builtin_amdgcn_permlane32_swap(__float_as_uint(x), __float_as_uint(x), false, false);
  return __uint_as_float(t[0]) + __uint_as_float(t[1]);
}

#define STAMP(k)
#define STAMP_DECL
#define STAMP_FLUSH(ws)
constexpr float SL2E = 0.088388347648318447f * 1.4426950408889634f;

constexpr size_t PART_OFF = 16u << 20, CNT_OFF = 8u << 20;
#define ASLEEP 0

#define G8_(j, off, sfx) "global_load_lds_dwordx4 %" #j ", %8 offset:" #off sfx "\n\t"
template <int MODE>
DI void glds8(const char* sbase, const unsigned (&v)[8], unsigned ldsaddr) {
  if (MODE == 0)
    asm volatile("s_mov_b32 m0, %9\n\ts_nop 0\n\t" G8_(0, -4096, "") G8_(1, -3072, "") G8_(2, -2048, "") G8_(3, -1024, "") G8_(4, 0, "") G8_(5, 1024, "") G8_(6, 2048, "") G8_(7, 3072, "")
                 ::"v"(v[0]), "v"(v[1]), "v"(v[2]), "v"(v[3]), "v"(v[4]), "v"(v[5]), "v"(v[6]), "v"(v[7]), "s"(sbase), "s"(ldsaddr) : "memory");
  else if (MODE == 3)
    asm volatile("s_mov_b32 m0, %9\n\ts_nop 0\n\t" G8_(0, -4096, " sc1") G8_(1, -3072, " sc1") G8_(2, -2048, " sc1") G8_(3, -1024, " sc1") G8_(4, 0, " sc1") G8_(5, 1024, " sc1") G8_(6, 2048, " sc1") G8_(7, 3072, " sc1")
                 ::"v"(v[0]), "v"(v[1]), "v"(v[2]), "v"(v[3]), "v"(v[4]), "v"(v[5]), "v"(v[6]), "v"(v[7]), "s"(sbase), "s"(ldsaddr) : "memory");
  else if (MODE == 1)
    asm volatile("s_mov_b32 m0, %9\n\ts_nop 0\n\t" G8_(0, -4096, " nt") G8_(1, -3072, " nt") G8_(2, -2048, " nt") G8_(3, -1024, " nt") G8_(4, 0, " nt") G8_(5, 1024, " nt") G8_(6, 2048, " nt") G8_(7, 3072, " nt")
                 ::"v"(v[0]), "v"(v[1]), "v"(v[2]), "v"(v[3]), "v"(v[4]), "v"(v[5]), "v"(v[6]), "v"(v[7]), "s"(sbase), "s"(ldsaddr) : "memory");
  else
    asm volatile("s_mov_b32 m0, %9\n\ts_nop 0\n\t" G8_(0, -4096, " nt") G8_(0, -4096, "") G8_(1, -3072, " nt") G8_(1, -3072, "") G8_(2, -2048, " nt") G8_(2, -2048, "") G8_(3, -1024, " nt") G8_(3, -1024, "")
                 G8_(4, 0, " nt") G8_(4, 0, "") G8_(5, 1024, " nt") G8_(5, 1024, "") G8_(6, 2048, " nt") G8_(6, 2048, "") G8_(7, 3072, " nt") G8_(7, 3072, "")
                 ::"v"(v[0]), "v"(v[1]), "v"(v[2]), "v"(v[3]), "v"(v[4]), "v"(v[5]), "v"(v[6]), "v"(v[7]), "s"(sbase), "s"(ldsaddr) : "memory");
}
#define CVAR 1
#define RPG 2
#define H0SLEEP 12
#define KVAPOL 0
#define BLKBAR 0
#define BLKBAR2 0
#define BNT 1
#define FNT 1
#define WOPF 1
#define FDUP 1
#define GSARG
#define GSPASS
#define GSPASS2
#define GBAR() BAR()
#define RLX_AGENT __ATOMIC_RELAXED, __HIP_MEMORY_SCOPE_AGENT
DI void poll_ge(unsigned* p, unsigned want) {
  unsigned spins = 0;
  while (__hip_atomic_load(p, RLX_AGENT) < want && ++spins < (1u << 22)) __builtin_amdgcn_s_sleep(1);
  asm volatile("" ::: "memory");
}
struct HFrags { s16x4 a[4][2]; bf16x8 b[4]; };
template <int WS, int XT, bool TAIL, class TailF>
DI void gemm_core(char* smem, unsigned lbase, const char* wg, const char* xg, int rot, int w, int l, f32x4 (&acc)[4][4], TailF tail, unsigned* gate GSARG) {
  int bn_ = 1; (void)bn_;
  const int c = l & 15, g = l >> 4, q4 = c >> 2, p = l & 3;
  if (w >= 4) {
    const int iw = w & 1;
    unsigned vw[8], vx[8];
    {
      const unsigned vw0 = (unsigned)(g * WS + ((c ^ (2 * g)) * 16)), vw1 = vw0 ^ 128;
      const unsigned vx0 = XT ? (unsigned)(l * 16) : (unsigned)((l >> 3) * 2048 + (((l & 7) ^ g) * 16));
      const unsigned vx1 = XT ? vx0 : (vx0 ^ 64);
#pragma unroll
      for (int j = 0; j < 8; ++j) {
        vw[j] = ((j & 2) ? vw1 : vw0) + j * (4 * WS - 1024);
        vx[j] = XT ? vx0 + (j >> 1) * (128 * 1024 - 2048) : ((j & 1) ? vx1 : vx0) + j * (16384 - 1024);
      }
    }
    const char* w_w = wg + (size_t)(32 * iw) * WS + 4096;
    const char* x_w = (XT ? xg + (size_t)(4 * iw) * (128 * 1024) : xg + (size_t)(64 * iw) * 2048) + 4096;
    const unsigned lw0 = lbase + iw * 8192 + 4096;
    auto wpieces = [&](int kap, unsigned bufoff, bool first) {
      if (first && FNT) glds8<FDUP ? 2 : 1>(w_w + (size_t)kap * (64 * WS), vw, lw0 + bufoff);
      else glds8<0>(w_w + (size_t)kap * (64 * WS), vw, lw0 + bufoff);
    };
    auto xpieces = [&](int kap, unsigned bufoff) { glds8<XT ? 3 : 0>(x_w + kap * (XT ? 2048 : 128), vx, lw0 + bufoff + 16384); };
    constexpr int FN = (FDUP && FNT) ? 24 : 16;
    if (w < 6) {
      if (XT) {
#pragma unroll 1
        for (int f = 0; f < 2; ++f) wpieces(rot + f, f * 32768, true);
        if (w == 4) poll_ge(gate, 8u);
        BAR();
#pragma unroll 1
        for (int f = 0; f < 2; ++f) xpieces(rot + f, f * 32768);
        WAIT_V(8);
      } else {
#pragma unroll 1
        for (int f = 0; f < 2; ++f) { wpieces(rot + f, f * 32768, true); xpieces(rot + f, f * 32768); }
      }
#pragma unroll 1
      for (int i = 0; i < 2; ++i) {
        WAIT_V(FN);
        GBAR();
        GBAR();
        wpieces(rot + i + 2, i * 32768, true); xpieces(rot + i + 2, i * 32768);
        GBAR();
        GBAR();
      }
      WAIT_V(FN);
      GBAR();
      GBAR();
      if (TAIL) tail(0);
      GBAR();
      if (TAIL) tail(1);
      GBAR();
      if (TAIL) tail(2);
      WAIT_V(TAIL ? 12 : 0);
      GBAR();
      if (TAIL) tail(3);
      GBAR();
      GBAR();
      GBAR();
    } else {
      auto stage = [&](int kap, int hb) { wpieces(kap & 15, 65536 + hb * 32768, false); xpieces(kap & 15, 65536 + hb * 32768); };
      __builtin_amdgcn_s_sleep(H0SLEEP);
      if (XT) { wpieces((rot + 4) & 15, 65536, false); BAR(); xpieces((rot + 4) & 15, 65536); }
      else stage(rot + 4, 0);
      GBAR();
      stage(rot + 8, 1);
      WAIT_V(16);
      GBAR();
      stage(rot + 12, 2);
#pragma unroll 1
      for (int i = 0; i < 3; ++i) {
        WAIT_V(16);
        GBAR();
        stage(rot + 5 + i, 0);
        WAIT_V(16);
        GBAR();
        stage(rot + 9 + i, 1);
        GBAR();
        stage(rot + 13 + i, 2);
        WAIT_V(32);
        GBAR();
      }
      if (TAIL) { tail(0); tail(1); }
      WAIT_V(TAIL ? 24 : 16);
      GBAR();
      if (TAIL) { tail(2); tail(3); }
      WAIT_V(TAIL ? 16 : 0);
      GBAR();
    }
  } else {
    if (XT) BAR();
    const int nh = w & 1, mh = w >> 1;
    unsigned aw[4];
    {
      const int F = 2 * q4 | 8 * (g & 1);
#pragma unroll
      for (int t = 0; t < 4; ++t) aw[t] = (unsigned)((8 * g + q4) * 256 + ((((2 * (4 * nh + t)) ^ F) + (p >> 1)) * 16) + (p & 1) * 8);
    }
    const unsigned ax0 = XT ? (unsigned)(16384 + mh * 8192 + l * 16) : (unsigned)(16384 + (64 * mh + c) * 128 + ((g ^ (c >> 1)) * 16));
    const unsigned ax1 = XT ? ax0 + 1024 : (ax0 ^ 64);
    struct FF { HFrags h[2]; };
    auto mm = [&](const FF& fo) {
#pragma unroll
      for (int ks = 0; ks < 2; ++ks)
#pragma unroll
        for (int t = 0; t < 4; ++t)
#pragma unroll
          for (int v = 0; v < 4; ++v) acc[t][v] = mfma16(cat8(fo.h[ks].a[t][0], fo.h[ks].a[t][1]), fo.h[ks].b[v], acc[t][v]);
    };
    auto full = [&](FF& fn, const FF& fo, unsigned bo) {
#pragma unroll
      for (int ks = 0; ks < 2; ++ks)
#pragma unroll
        for (int t = 0; t < 4; ++t) {
          fn.h[ks].b[t] = lds_b128(smem, (ks ? ax1 : ax0) + bo + t * 2048);
          fn.h[ks].a[t][0] = lds_tr(smem, aw[t] + bo + ks * 8192);
          fn.h[ks].a[t][1] = lds_tr(smem, aw[t] + bo + ks * 8192 + 1024);
        }
      mm(fo);
#pragma unroll
      for (int t = 0; t < 24; ++t) {
        __builtin_amdgcn_sched_group_barrier(0x008, 1, 0);
        __builtin_amdgcn_sched_group_barrier(0x100, 1, 0);
      }
      __builtin_amdgcn_sched_group_barrier(0x008, 8, 0);
      SB();
    };
#pragma unroll
    for (int t = 0; t < 4; ++t)
#pragma unroll
      for (int v = 0; v < 4; ++v) acc[t][v] = f32x4{0, 0, 0, 0};
    FF f0, f1;
#pragma unroll
    for (int ks = 0; ks < 2; ++ks)
#pragma unroll
      for (int t = 0; t < 4; ++t) { f1.h[ks].b[t] = bf16x8{0, 0, 0, 0, 0, 0, 0, 0}; f1.h[ks].a[t][0] = s16x4{0, 0, 0, 0}; f1.h[ks].a[t][1] = s16x4{0, 0, 0, 0}; }
#pragma unroll 1
    for (int n = 0; n < 16; n += 2) {
      const unsigned bo0 = (n & 2) ? 98304 : ((n >> 2) & 1) * 32768, bo1 = (n & 2) ? 131072 : 65536;
      WAIT_L0();
      GBAR();
      full(f0, f1, bo0);
      WAIT_L0();
      GBAR();
      full(f1, f0, bo1);
    }
    WAIT_L0();
    mm(f1);
  }
}

__global__ __launch_bounds__(512, 2) void k1_qproj_attn(const char* __restrict__ xA, const char* __restrict__ kA, const char* __restrict__ vA,
                                                        const char* __restrict__ wqA, const char* __restrict__ xB, const char* __restrict__ kB,
                                                        const char* __restrict__ vB, const char* __restrict__ wqB, char* __restrict__ ows,
                                                        const char* __restrict__ woA, const char* __restrict__ woB, char* __restrict__ out) {
  __shared__ __attribute__((aligned(1024))) char smem[163840];
  const unsigned lbase = (unsigned)(size_t)(LAS char*)smem;
  const int tid = threadIdx.x, l = tid & 63, w = __builtin_amdgcn_readfirstlane(tid >> 6);
  const int xcd = blockIdx.x & 7, j = blockIdx.x >> 3;
  const int br = j & 1, idx = j >> 1;
  int head, bg, kvh, hkv, rot, brot;
  if (br) { const int a = idx >> 2; bg = idx & 3; head = 4 * xcd + a; kvh = head; hkv = 32; rot = 4 * ((a + bg) & 3); brot = 0; }
  else { const int a = idx >> 1; kvh = xcd >> 1; head = 8 * kvh + a; bg = (xcd & 1) * 2 + (idx & 1); hkv = 4; rot = 4 * ((a + 2 * (idx & 1)) & 3); brot = a & 3; }
  const char* xg = (br ? xB : xA) + (size_t)bg * 128 * 2048;
  const char* wq = (br ? wqB : wqA) + head * 256;
  const int c = l & 15, g = l >> 4, q4 = c >> 2, p = l & 3;
  const int u = w & 3, dh = w >> 2;
  const unsigned ring = (unsigned)((w >= 4 ? w - 4 : w < 2 ? w + 4 : w + 6) * 16384);
  STAMP_DECL;
  STAMP(0);
  if (ASLEEP && !br) {
#pragma unroll 1
    for (int i = 0; i < ASLEEP; ++i) __builtin_amdgcn_s_sleep(16);
  }
  const int b = 4 * bg + u;
  const size_t kvoff = ((size_t)(b * hkv + kvh) * 512 + dh * 256) * 256;
  const char* kg = (br ? kB : kA) + kvoff;
  const char* vg = (br ? vB : vA) + kvoff;
  unsigned vk[4], vv[2];
#pragma unroll
  for (int i = 0; i < 4; ++i) vk[i] = (unsigned)(g * 256 + ((c ^ g ^ (4 * i)) * 16));
#pragma unroll
  for (int i = 0; i < 2; ++i) vv[i] = (unsigned)(g * 256 + ((c ^ (2 * g) ^ (8 * i)) * 16));
  auto slot_off = [](int t) constexpr { return t * 4096; };
  auto tile_issue = [&](int blk, int isV, int t) {
    const unsigned la = lbase + slot_off(t) + ring;
    const char* src = (isV ? vg : kg) + (((blk + brot) & 3) * 4 + t) * 4096;
    if (br && BNT) {
      if (isV) glds4<true>(src, vv[0], vv[1], vv[0], vv[1], la);
      else glds4<true>(src, vk[0], vk[1], vk[2], vk[3], la);
    } else {
      if (!br && KVAPOL && blk == 0 && (KVAPOL == 2 || idx < 8)) {
        if (isV) glds4<true>(src, vv[0], vv[1], vv[0], vv[1], la);
        else glds4<true>(src, vk[0], vk[1], vk[2], vk[3], la);
      }
      if (isV) glds4<false>(src, vv[0], vv[1], vv[0], vv[1], la);
      else glds4<false>(src, vk[0], vk[1], vk[2], vk[3], la);
    }
  };

  unsigned* ctl = (unsigned*)(ows + CNT_OFF);
  const __amdgpu_buffer_rsrc_t rc = __builtin_amdgcn_make_buffer_rsrc(ows + CNT_OFF, 0, 65536 + 16384, 0x00020000);
  unsigned nonce = 0;
  auto echo = [&]() {
    const u32x4 r = __builtin_amdgcn_raw_buffer_load_b128(rc, l * 16, 0, 16);
    __builtin_amdgcn_raw_buffer_store_b128(r, rc, 4096 + l * 16, 0, 16);
  };
  if (w == 0) {
    nonce = __hip_atomic_load(ctl + 1024 + blockIdx.x, RLX_AGENT) + 1u;
    if (l == 0) __hip_atomic_store(ctl + blockIdx.x, nonce, RLX_AGENT);
  } else if (w == 1 && blockIdx.x == 0) {
#pragma unroll
    for (int i = 0; i < 5; ++i) __builtin_amdgcn_raw_buffer_store_b32(0u, rc, 8192 + (i * 64 + l) * 128, 0, 16);
    WAIT_V(0);
    __builtin_amdgcn_s_sleep(24);
    echo();
  }
  f32x4 qa[4][4];
  gemm_core<8192, 0, true>(smem, lbase, wq, xg, rot, w, l, qa, [&](int n) { tile_issue(0, 0, n); }, nullptr GSPASS);
  if (w < 4) {
#pragma unroll
    for (int t = 0; t < 4; ++t)
#pragma unroll
      for (int v = 0; v < 4; ++v) {
        const int tau = 4 * (w & 1) + t;
        u32x2 pkd = {pk2(qa[t][v][0], qa[t][v][1]), pk2(qa[t][v][2], qa[t][v][3])};
        const unsigned off = 98304 + ((w >> 1) * 4 + v) * 4096 + (tau >> 1) * 1024 + ((2 * (tau & 1) + (g >> 1)) * 16 + c) * 16 + (g & 1) * 8;
        *(u32x2*)(smem + off) = pkd;
      }
  }
  WAIT_L0();
  BAR();
  bf16x8 qf[2][4];
#pragma unroll
  for (int v = 0; v < 2; ++v)
#pragma unroll
    for (int s = 0; s < 4; ++s) qf[v][s] = lds_b128(smem, 98304 + u * 8192 + v * 4096 + s * 1024 + l * 16);
  if (w == 1 && blockIdx.x == 0) { echo(); WAIT_V(0); }
  if (w == 0) {
    unsigned spins = 0;
    while (__hip_atomic_load(ctl + 1024 + blockIdx.x, RLX_AGENT) != nonce && ++spins < (1u << 22)) __builtin_amdgcn_s_sleep(2);
    if (l == 0) __hip_atomic_fetch_add(ctl + 2048, 1u, RLX_AGENT);
  }
  if (w < 4) {
#pragma unroll 1
    for (int t = 0; t < 4; ++t) tile_issue(0, 0, t);
  }
  STAMP(1);
#define STAMPA(k) STAMP(k)

  unsigned ak[4], av[8];
#pragma unroll
  for (int s = 0; s < 4; ++s) ak[s] = (unsigned)(ring + c * 256 + ((g ^ c ^ (4 * s)) * 16));
  {
    const int row = 4 * g + q4, F = 2 * (row & 7);
#pragma unroll
    for (int t = 0; t < 8; ++t) av[t] = (unsigned)(ring + row * 256 + ((((2 * t) ^ F) + (p >> 1)) * 16) + (p & 1) * 8);
  }
  f32x4 o[8][2];
#pragma unroll
  for (int t = 0; t < 8; ++t) { o[t][0] = f32x4{0, 0, 0, 0}; o[t][1] = f32x4{0, 0, 0, 0}; }
  float m[2] = {-__builtin_inff(), -__builtin_inff()}, ls[2] = {0.f, 0.f};
#pragma unroll 1
  for (int blk = 0; blk < 4; ++blk) {
    f32x4 sc[4][2];
#pragma unroll
    for (int t = 0; t < 4; ++t) {
      WAIT_V(12);
      bf16x8 kf[4];
#pragma unroll
      for (int s = 0; s < 4; ++s) kf[s] = lds_b128(smem, ak[s] + slot_off(t));
      WAIT_L0();
      tile_issue(blk, 1, t);
      sc[t][0] = f32x4{0, 0, 0, 0}; sc[t][1] = f32x4{0, 0, 0, 0};
#pragma unroll
      for (int s = 0; s < 4; ++s)
#pragma unroll
        for (int v = 0; v < 2; ++v) sc[t][v] = mfma16(kf[s], qf[v][s], sc[t][v]);
      SB();
    }
    bf16x8 pf[2][2];
#pragma unroll
    for (int v = 0; v < 2; ++v) {
      float mx = sc[0][v][0];
#pragma unroll
      for (int t = 0; t < 4; ++t)
#pragma unroll
        for (int i = 0; i < 4; ++i) mx = fmaxf(mx, sc[t][v][i]);
      mx = xg_max(mx) * SL2E;
      const float mn = fmaxf(m[v], mx);
      const float alpha = __builtin_amdgcn_exp2f(m[v] - mn);
      ls[v] *= alpha;
#pragma unroll
      for (int t = 0; t < 8; ++t) o[t][v] *= alpha;
      m[v] = mn;
      float sum = 0.f;
#pragma unroll
      for (int t = 0; t < 4; ++t)
#pragma unroll
        for (int i = 0; i < 4; ++i) { const float e = __builtin_amdgcn_exp2f(fmaf(sc[t][v][i], SL2E, -mn)); sc[t][v][i] = e; sum += e; }
      ls[v] += sum;
#pragma unroll
      for (int kb = 0; kb < 2; ++kb) {
        u32x4 pw = {pk2(sc[2 * kb][v][0], sc[2 * kb][v][1]), pk2(sc[2 * kb][v][2], sc[2 * kb][v][3]),
                    pk2(sc[2 * kb + 1][v][0], sc[2 * kb + 1][v][1]), pk2(sc[2 * kb + 1][v][2], sc[2 * kb + 1][v][3])};
        pf[kb][v] = __builtin_bit_cast(bf16x8, pw);
      }
    }
    const bool more = blk < 3;
    if (BLKBAR2 == 2 || (BLKBAR2 == 1 && !br)) BAR();
#pragma unroll
    for (int kb = 0; kb < 2; ++kb) {
      s16x4 vlo[8], vhi[8];
      if (kb == 0 || more) WAIT_V(12); else WAIT_V(4);
#pragma unroll
      for (int t = 0; t < 8; ++t) vlo[t] = lds_tr(smem, av[t] + slot_off(2 * kb));
      WAIT_L0();
      if (more) { tile_issue(blk + 1, 0, 2 * kb); WAIT_V(12); }
      else if (kb == 0) WAIT_V(8);
      else WAIT_V(0);
#pragma unroll
      for (int t = 0; t < 8; ++t) vhi[t] = lds_tr(smem, av[t] + slot_off(2 * kb + 1));
      WAIT_L0();
      if (more) tile_issue(blk + 1, 0, 2 * kb + 1);
#pragma unroll
      for (int t = 0; t < 8; ++t)
#pragma unroll
        for (int v = 0; v < 2; ++v) o[t][v] = mfma16(cat8(vlo[t], vhi[t]), pf[kb][v], o[t][v]);
      SB();
    }
    if (BLKBAR == 2 || (BLKBAR == 1 && !br)) BAR();
    STAMPA(2 + blk);
  }
  ls[0] = xg_sum(ls[0]);
  ls[1] = xg_sum(ls[1]);
  if (dh) {
#pragma unroll
    for (int t = 0; t < 8; ++t) { const f32x4 tmp = o[t][0]; o[t][0] = o[t][1]; o[t][1] = tmp; }
    float tm = m[0]; m[0] = m[1]; m[1] = tm;
    tm = ls[0]; ls[0] = ls[1]; ls[1] = tm;
  }
  {
#pragma unroll
    for (int t = 0; t < 8; ++t) *(f32x4*)(smem + ring + t * 1024 + l * 16) = o[t][1];
    f32x2 ml = {m[1], ls[1]};
    *(f32x2*)(smem + ring + 8192 + l * 8) = ml;
  }
  WAIT_L0();
  BAR();
  {
    const int pw = w ^ 4;
    const unsigned pring = (unsigned)((pw >= 4 ? pw - 4 : pw < 2 ? pw + 4 : pw + 6) * 16384);
    const f32x2 ml = *(const f32x2*)(smem + pring + 8192 + l * 8);
    const float mo = m[0], lo = ls[0];
    const float mm = fmaxf(mo, ml[0]);
    float a0 = __builtin_amdgcn_exp2f(mo - mm), a1 = __builtin_amdgcn_exp2f(ml[0] - mm);
    const float inv = 1.0f / (lo * a0 + ml[1] * a1);
    a0 *= inv; a1 *= inv;
    const __amdgpu_buffer_rsrc_t ro = __builtin_amdgcn_make_buffer_rsrc(ows + (size_t)br * (4u << 20) + ((size_t)(2 * b + dh) * 128 + head * 4) * 1024, 0, 4096, 0x00020000);
#pragma unroll
    for (int t = 0; t < 8; ++t) {
      const f32x4 oth = *(const f32x4*)(smem + pring + t * 1024 + l * 16);
      const f32x4 r = o[t][0] * a0 + oth * a1;
      u32x2 pkd = {pk2(r[0], r[1]), pk2(r[2], r[3])};
      __builtin_amdgcn_raw_buffer_store_b64(pkd, ro, t * 512 + ((g >> 1) * 16 + c) * 16 + (g & 1) * 8, 0, 16);
    }
  }
  if (w == 1 && blockIdx.x == 0) {
    unsigned spins = 0;
    while (__hip_atomic_load(ctl + 2048, RLX_AGENT) < 256u && ++spins < (1u << 17)) { echo(); __builtin_amdgcn_s_sleep(8); }
  }
  WAIT_V(0);
  WAIT_L0();
  BAR();
  unsigned* cnt = ctl + 2048 + 32;
  if (tid == 0) __hip_atomic_fetch_add(cnt + ((br * 4 + bg) * 4 + (head >> 3)) * 32, 1u, RLX_AGENT);
  STAMPA(6);

  {
    const int kq = xcd >> 1, mt = idx & 3, nl = idx >> 2, nt = (xcd & 1) * 4 + nl;
    if (WOPF && !br && w < 4) {
      unsigned vp[8];
#pragma unroll
      for (int i = 0; i < 8; ++i) vp[i] = l * 16 + i * 1024;
      const char* src = woB + (size_t)(kq * 1024 + idx * 64 + w * 16) * 2048 + (xcd & 1) * 1024 + 4096;
      glds8<2>(src, vp, lbase + 98304 + w * 16384 + 4096);
      glds8<2>(src + 8 * 2048, vp, lbase + 98304 + w * 16384 + 8192 + 4096);
      WAIT_V(0);
    }
    const char* og = ows + (size_t)br * (4u << 20) + ((size_t)(mt * 8) * 128 + kq * 32) * 1024;
    const char* wo = (br ? woB : woA) + (size_t)(kq * 1024) * 2048 + nt * 256;
    f32x4 acc[4][4];
    gemm_core<2048, 1, false>(smem, lbase, wo, og, 4 * ((mt + nl) & 3), w, l, acc, [](int) {}, cnt + ((br * 4 + mt) * 4 + kq) * 32 GSPASS2);
    STAMPA(7);
    const int tile = br * 32 + nt * 4 + mt;
    const __amdgpu_buffer_rsrc_t rs = __builtin_amdgcn_make_buffer_rsrc(ows + PART_OFF + (size_t)tile * (16 * 16384), 0, 16 * 16384, 0x00020000);
    unsigned* rcnt = cnt + (32 + tile * 4) * 32;
    if (w < 4) {
      if (w != kq) {
        const int off = (w * 4 + kq) * 16384 + l * 16;
#pragma unroll
        for (int t = 0; t < 4; ++t)
#pragma unroll
          for (int v = 0; v < 4; ++v) __builtin_amdgcn_raw_buffer_store_b128(__builtin_bit_cast(u32x4, acc[t][v]), rs, off + (t * 4 + v) * 1024, 0, 16);
        WAIT_V(0);
        if (l == 0) __hip_atomic_fetch_add(rcnt + w * 32, 1u, RLX_AGENT);
      } else {
#pragma unroll
        for (int t = 0; t < 4; ++t)
#pragma unroll
          for (int v = 0; v < 4; ++v) *(f32x4*)(smem + (t * 4 + v) * 1024 + l * 16) = acc[t][v];
        WAIT_L0();
      }
    } else if (w == 4) {
      poll_ge(rcnt + kq * 32, 3u);
    }
    asm volatile("" ::: "memory");
    BAR();
    asm volatile("" ::: "memory");
    u32x4 pr[3][2];
#pragma unroll
    for (int s = 0; s < 3; ++s) {
      const int src = s + (s >= kq);
#pragma unroll
      for (int i = 0; i < 2; ++i) pr[s][i] = __builtin_amdgcn_raw_buffer_load_b128(rs, (kq * 4 + src) * 16384 + (2 * w + i) * 1024 + l * 16, 0, 16);
    }
#pragma unroll
    for (int i = 0; i < 2; ++i) {
      const f32x4 own = *(const f32x4*)(smem + (2 * w + i) * 1024 + l * 16);
      const f32x4 r = (__builtin_bit_cast(f32x4, pr[0][i]) + __builtin_bit_cast(f32x4, pr[1][i])) + (__builtin_bit_cast(f32x4, pr[2][i]) + own);
      const int mm = mt * 128 + (kq >> 1) * 64 + ((w & 1) * 2 + i) * 16 + c, nn = nt * 128 + (kq & 1) * 64 + (w >> 1) * 16 + g * 4;
      u32x2 pkd = {pk2(r[0], r[1]), pk2(r[2], r[3])};
      *(u32x2*)(out + ((size_t)mm * 2048 + br * 1024 + nn) * 2) = pkd;
    }
  }
  STAMPA(8);
  STAMP_FLUSH(ows);
}

extern "C" void kernel_launch(void* const* d_in, const int* in_sizes, int n_in, void* d_out, int out_size, void* d_ws, size_t ws_size,
                              hipStream_t stream) {
  k1_qproj_attn<<<256, 512, 0, stream>>>((const char*)d_in[0], (const char*)d_in[1], (const char*)d_in[2], (const char*)d_in[3], (const char*)d_in[5],
                                         (const char*)d_in[6], (const char*)d_in[7], (const char*)d_in[8], (char*)d_ws, (const char*)d_in[4],
                                         (const char*)d_in[9], (char*)d_out);
}
```

```cpp
#include <hip/hip_runtime.h>
#include <hip/hip_bf16.h>

#define POL_Q 2
#define POL_KVA 2

typedef short bf16x8 __attribute__((ext_vector_type(8)));
typedef short s16x4 __attribute__((ext_vector_type(4)));
typedef float f32x4 __attribute__((ext_vector_type(4)));
typedef float f32x2 __attribute__((ext_vector_type(2)));
typedef __bf16 bf16x2 __attribute__((ext_vector_type(2)));
typedef unsigned u32x4 __attribute__((ext_vector_type(4)));
typedef unsigned u32x2 __attribute__((ext_vector_type(2)));
#define LAS __attribute__((address_space(3)))
#define DI __device__ __forceinline__

#define WAIT_V(n) asm volatile("s_waitcnt vmcnt(%0)" ::"n"(n) : "memory")
#define WAIT_L0() do { asm volatile("" ::: "memory"); __builtin_amdgcn_s_waitcnt(0xC07F); asm volatile("" ::: "memory"); } while (0)
#define BAR() __builtin_amdgcn_s_barrier()
#define SB() __builtin_amdgcn_sched_barrier(0)

template <bool NT>
DI void glds(const char* sbase, unsigned voff, unsigned ldsaddr) {
  if (NT)
    asm volatile("s_mov_b32 m0, %2\n\ts_nop 0\n\tglobal_load_lds_dwordx4 %0, %1 nt" ::"v"(voff), "s"(sbase), "s"(ldsaddr) : "memory");
  else
    asm volatile("s_mov_b32 m0, %2\n\ts_nop 0\n\tglobal_load_lds_dwordx4 %0, %1" ::"v"(voff), "s"(sbase), "s"(ldsaddr) : "memory");
}
template <bool NT>
DI void glds4(const char* sbase, unsigned v0, unsigned v1, unsigned v2, unsigned v3, unsigned ldsaddr) {
  if (NT)
    asm volatile("s_mov_b32 m0, %5\n\ts_nop 0\n\t"
                 "global_load_lds_dwordx4 %0, %4 nt\n\t"
                 "global_load_lds_dwordx4 %1, %4 offset:1024 nt\n\t"
                 "global_load_lds_dwordx4 %2, %4 offset:2048 nt\n\t"
                 "global_load_lds_dwordx4 %3, %4 offset:3072 nt"
                 ::"v"(v0), "v"(v1), "v"(v2), "v"(v3), "s"(sbase), "s"(ldsaddr) : "memory");
  else
    asm volatile("s_mov_b32 m0, %5\n\ts_nop 0\n\t"
                 "global_load_lds_dwordx4 %0, %4\n\t"
                 "global_load_lds_dwordx4 %1, %4 offset:1024\n\t"
                 "global_load_lds_dwordx4 %2, %4 offset:2048\n\t"
                 "global_load_lds_dwordx4 %3, %4 offset:3072"
                 ::"v"(v0), "v"(v1), "v"(v2), "v"(v3), "s"(sbase), "s"(ldsaddr) : "memory");
}
DI void pin(const char* sbase, unsigned voff, unsigned ldsaddr) {
  asm volatile("s_mov_b32 m0, %2\n\ts_nop 0\n\tglobal_load_lds_dword %0, %1" ::"v"(voff), "s"(sbase), "s"(ldsaddr) : "memory");
}

DI f32x4 mfma16(bf16x8 a, bf16x8 b, f32x4 c) { return __builtin_amdgcn_mfma_f32_16x16x32_bf16(a, b, c, 0, 0, 0); }
DI unsigned pk2(float lo, float hi) { f32x2 a = {lo, hi}; return __builtin_bit_cast(unsigned, __builtin_convertvector(a, bf16x2)); }
DI bf16x8 lds_b128(const char* smem, unsigned off) { return *(const bf16x8*)(smem + off); }
DI s16x4 lds_tr(char* smem, unsigned off) { return __builtin_amdgcn_ds_read_tr16_b64_v4i16((LAS s16x4*)(smem + off)); }
DI bf16x8 cat8(s16x4 lo, s16x4 hi) { return __builtin_shufflevector(lo, hi, 0, 1, 2, 3, 4, 5, 6, 7); }
DI float xg_max(float x) {
  auto s = __builtin_amdgcn_permlane16_swap(__float_as_uint(x), __float_as_uint(x), false, false);
  x = fmaxf(__uint_as_float(s[0]), __uint_as_float(s[1]));
  auto t = __builtin_amdgcn_permlane32_swap(__float_as_uint(x), __float_as_uint(x), false, false);
  return fmaxf(__uint_as_float(t[0]), __uint_as_float(t[1]));
}
DI float xg_sum(float x) {
  auto s = __builtin_amdgcn_permlane16_swap(__float_as_uint(x), __float_as_uint(x), false, false);
  x = __uint_as_float(s[0]) + __uint_as_float(s[1]);
  auto t = __builtin_amdgcn_permlane32_swap(__float_as_uint(x), __float_as_uint(x), false, false);
  return __uint_as_float(t[0]) + __uint_as_float(t[1]);
}

#define STAMP(k)
#define STAMP_DECL
#define STAMP_FLUSH(ws)
constexpr float SL2E = 0.088388347648318447f * 1.4426950408889634f;

constexpr size_t PART_OFF = 16u << 20, CNT_OFF = 8u << 20, Q_OFF = 10u << 20;
#define ASLEEP 0

#define G8_(j, off, sfx) "global_load_lds_dwordx4 %" #j ", %8 offset:" #off sfx "\n\t"
template <int MODE>
DI void glds8(const char* sbase, const unsigned (&v)[8], unsigned ldsaddr) {
  if (MODE == 0)
    asm volatile("s_mov_b32 m0, %9\n\ts_nop 0\n\t" G8_(0, -4096, "") G8_(1, -3072, "") G8_(2, -2048, "") G8_(3, -1024, "") G8_(4, 0, "") G8_(5, 1024, "") G8_(6, 2048, "") G8_(7, 3072, "")
                 ::"v"(v[0]), "v"(v[1]), "v"(v[2]), "v"(v[3]), "v"(v[4]), "v"(v[5]), "v"(v[6]), "v"(v[7]), "s"(sbase), "s"(ldsaddr) : "memory");
  else if (MODE == 3)
    asm volatile("s_mov_b32 m0, %9\n\ts_nop 0\n\t" G8_(0, -4096, " sc1") G8_(1, -3072, " sc1") G8_(2, -2048, " sc1") G8_(3, -1024, " sc1") G8_(4, 0, " sc1") G8_(5, 1024, " sc1") G8_(6, 2048, " sc1") G8_(7, 3072, " sc1")
                 ::"v"(v[0]), "v"(v[1]), "v"(v[2]), "v"(v[3]), "v"(v[4]), "v"(v[5]), "v"(v[6]), "v"(v[7]), "s"(sbase), "s"(ldsaddr) : "memory");
  else if (MODE == 1)
    asm volatile("s_mov_b32 m0, %9\n\ts_nop 0\n\t" G8_(0, -4096, " nt") G8_(1, -3072, " nt") G8_(2, -2048, " nt") G8_(3, -1024, " nt") G8_(4, 0, " nt") G8_(5, 1024, " nt") G8_(6, 2048, " nt") G8_(7, 3072, " nt")
                 ::"v"(v[0]), "v"(v[1]), "v"(v[2]), "v"(v[3]), "v"(v[4]), "v"(v[5]), "v"(v[6]), "v"(v[7]), "s"(sbase), "s"(ldsaddr) : "memory");
  else
    asm volatile("s_mov_b32 m0, %9\n\ts_nop 0\n\t" G8_(0, -4096, " nt") G8_(0, -4096, "") G8_(1, -3072, " nt") G8_(1, -3072, "") G8_(2, -2048, " nt") G8_(2, -2048, "") G8_(3, -1024, " nt") G8_(3, -1024, "")
                 G8_(4, 0, " nt") G8_(4, 0, "") G8_(5, 1024, " nt") G8_(5, 1024, "") G8_(6, 2048, " nt") G8_(6, 2048, "") G8_(7, 3072, " nt") G8_(7, 3072, "")
                 ::"v"(v[0]), "v"(v[1]), "v"(v[2]), "v"(v[3]), "v"(v[4]), "v"(v[5]), "v"(v[6]), "v"(v[7]), "s"(sbase), "s"(ldsaddr) : "memory");
}
#define CVAR 1
#define RPG 2
#define H0SLEEP 12
#define KVAPOL 0
#define BLKBAR 0
#define BLKBAR2 0
#define BNT 1
#define FNT 1
#define WOPF 1
#define FDUP 1
#define GSARG
#define GSPASS
#define GSPASS2
#define GBAR() BAR()
#define RLX_AGENT __ATOMIC_RELAXED, __HIP_MEMORY_SCOPE_AGENT
DI void poll_ge(unsigned* p, unsigned want) {
  unsigned spins = 0;
  while (__hip_atomic_load(p, RLX_AGENT) < want && ++spins < (1u << 22)) __builtin_amdgcn_s_sleep(1);
  asm volatile("" ::: "memory");
}
struct HFrags { s16x4 a[4][2]; bf16x8 b[4]; };
template <int WS, int XT, bool TAIL, class TailF>
DI void gemm_core(char* smem, unsigned lbase, const char* wg, const char* xg, int rot, int w, int l, f32x4 (&acc)[4][4], TailF tail, bool dotail, unsigned* gate GSARG) {
  int bn_ = 1; (void)bn_;
  const int c = l & 15, g = l >> 4, q4 = c >> 2, p = l & 3;
  if (w >= 4) {
    const int iw = w & 1;
    unsigned vw[8], vx[8];
    {
      const unsigned vw0 = (unsigned)(g * WS + ((c ^ (2 * g)) * 16)), vw1 = vw0 ^ 128;
      const unsigned vx0 = XT ? (unsigned)(l * 16) : (unsigned)((l >> 3) * 2048 + (((l & 7) ^ g) * 16));
      const unsigned vx1 = XT ? vx0 : (vx0 ^ 64);
#pragma unroll
      for (int j = 0; j < 8; ++j) {
        vw[j] = ((j & 2) ? vw1 : vw0) + j * (4 * WS - 1024);
        vx[j] = XT ? vx0 + (j >> 1) * (128 * 1024 - 2048) : ((j & 1) ? vx1 : vx0) + j * (16384 - 1024);
      }
    }
    const char* w_w = wg + (size_t)(32 * iw) * WS + 4096;
    const char* x_w = (XT ? xg + (size_t)(4 * iw) * (128 * 1024) : xg + (size_t)(64 * iw) * 2048) + 4096;
    const unsigned lw0 = lbase + iw * 8192 + 4096;
    auto wpieces = [&](int kap, unsigned bufoff, bool first) {
      if (first && FNT) glds8<FDUP ? 2 : 1>(w_w + (size_t)kap * (64 * WS), vw, lw0 + bufoff);
      else glds8<0>(w_w + (size_t)kap * (64 * WS), vw, lw0 + bufoff);
    };
    auto xpieces = [&](int kap, unsigned bufoff) { glds8<XT ? 3 : 0>(x_w + kap * (XT ? 2048 : 128), vx, lw0 + bufoff + 16384); };
    constexpr int FN = (FDUP && FNT) ? 24 : 16;
    if (w < 6) {
      if (XT) {
#pragma unroll 1
        for (int f = 0; f < 2; ++f) wpieces(rot + f, f * 32768, true);
        if (w == 4) poll_ge(gate, 8u);
        BAR();
#pragma unroll 1
        for (int f = 0; f < 2; ++f) xpieces(rot + f, f * 32768);
        WAIT_V(8);
      } else {
#pragma unroll 1
        for (int f = 0; f < 2; ++f) { wpieces(rot + f, f * 32768, true); xpieces(rot + f, f * 32768); }
      }
#pragma unroll 1
      for (int i = 0; i < 2; ++i) {
        WAIT_V(FN);
        GBAR();
        GBAR();
        wpieces(rot + i + 2, i * 32768, true); xpieces(rot + i + 2, i * 32768);
        GBAR();
        GBAR();
      }
      WAIT_V(FN);
      GBAR();
      GBAR();
      if (TAIL && dotail) tail(0);
      GBAR();
      if (TAIL && dotail) tail(1);
      GBAR();
      if (TAIL && dotail) { tail(2); WAIT_V(12); } else WAIT_V(0);
      GBAR();
      if (TAIL && dotail) tail(3);
      GBAR();
      GBAR();
      GBAR();
    } else {
      auto stage = [&](int kap, int hb) { wpieces(kap & 15, 65536 + hb * 32768, false); xpieces(kap & 15, 65536 + hb * 32768); };
      __builtin_amdgcn_s_sleep(H0SLEEP);
      if (XT) { wpieces((rot + 4) & 15, 65536, false); BAR(); xpieces((rot + 4) & 15, 65536); }
      else stage(rot + 4, 0);
      GBAR();
      stage(rot + 8, 1);
      WAIT_V(16);
      GBAR();
      stage(rot + 12, 2);
#pragma unroll 1
      for (int i = 0; i < 3; ++i) {
        WAIT_V(16);
        GBAR();
        stage(rot + 5 + i, 0);
        WAIT_V(16);
        GBAR();
        stage(rot + 9 + i, 1);
        GBAR();
        stage(rot + 13 + i, 2);
        WAIT_V(32);
        GBAR();
      }
      if (TAIL && dotail) { tail(0); tail(1); WAIT_V(24); } else WAIT_V(16);
      GBAR();
      if (TAIL && dotail) { tail(2); tail(3); WAIT_V(16); } else WAIT_V(0);
      GBAR();
    }
  } else {
    if (XT) BAR();
    const int nh = w & 1, mh = w >> 1;
    unsigned aw[4];
    {
      const int F = 2 * q4 | 8 * (g & 1);
#pragma unroll
      for (int t = 0; t < 4; ++t) aw[t] = (unsigned)((8 * g + q4) * 256 + ((((2 * (4 * nh + t)) ^ F) + (p >> 1)) * 16) + (p & 1) * 8);
    }
    const unsigned ax0 = XT ? (unsigned)(16384 + mh * 8192 + l * 16) : (unsigned)(16384 + (64 * mh + c) * 128 + ((g ^ (c >> 1)) * 16));
    const unsigned ax1 = XT ? ax0 + 1024 : (ax0 ^ 64);
    struct FF { HFrags h[2]; };
    auto mm = [&](const FF& fo) {
#pragma unroll
      for (int ks = 0; ks < 2; ++ks)
#pragma unroll
        for (int t = 0; t < 4; ++t)
#pragma unroll
          for (int v = 0; v < 4; ++v) acc[t][v] = mfma16(cat8(fo.h[ks].a[t][0], fo.h[ks].a[t][1]), fo.h[ks].b[v], acc[t][v]);
    };
    auto full = [&](FF& fn, const FF& fo, unsigned bo) {
#pragma unroll
      for (int ks = 0; ks < 2; ++ks)
#pragma unroll
        for (int t = 0; t < 4; ++t) {
          fn.h[ks].b[t] = lds_b128(smem, (ks ? ax1 : ax0) + bo + t * 2048);
          fn.h[ks].a[t][0] = lds_tr(smem, aw[t] + bo + ks * 8192);
          fn.h[ks].a[t][1] = lds_tr(smem, aw[t] + bo + ks * 8192 + 1024);
        }
      mm(fo);
#pragma unroll
      for (int t = 0; t < 24; ++t) {
        __builtin_amdgcn_sched_group_barrier(0x008, 1, 0);
        __builtin_amdgcn_sched_group_barrier(0x100, 1, 0);
      }
      __builtin_amdgcn_sched_group_barrier(0x008, 8, 0);
      SB();
    };
#pragma unroll
    for (int t = 0; t < 4; ++t)
#pragma unroll
      for (int v = 0; v < 4; ++v) acc[t][v] = f32x4{0, 0, 0, 0};
    FF f0, f1;
#pragma unroll
    for (int ks = 0; ks < 2; ++ks)
#pragma unroll
      for (int t = 0; t < 4; ++t) { f1.h[ks].b[t] = bf16x8{0, 0, 0, 0, 0, 0, 0, 0}; f1.h[ks].a[t][0] = s16x4{0, 0, 0, 0}; f1.h[ks].a[t][1] = s16x4{0, 0, 0, 0}; }
#pragma unroll 1
    for (int n = 0; n < 16; n += 2) {
      const unsigned bo0 = (n & 2) ? 98304 : ((n >> 2) & 1) * 32768, bo1 = (n & 2) ? 131072 : 65536;
      WAIT_L0();
      GBAR();
      full(f0, f1, bo0);
      WAIT_L0();
      GBAR();
      full(f1, f0, bo1);
    }
    WAIT_L0();
    mm(f1);
  }
}

__global__ __launch_bounds__(512, 2) void k1_qproj_attn(const char* __restrict__ xA, const char* __restrict__ kA, const char* __restrict__ vA,
                                                        const char* __restrict__ wqA, const char* __restrict__ xB, const char* __restrict__ kB,
                                                        const char* __restrict__ vB, const char* __restrict__ wqB, char* __restrict__ ows,
                                                        const char* __restrict__ woA, const char* __restrict__ woB, char* __restrict__ out) {
  __shared__ __attribute__((aligned(1024))) char smem[163840];
  const unsigned lbase = (unsigned)(size_t)(LAS char*)smem;
  const int tid = threadIdx.x, l = tid & 63, w = __builtin_amdgcn_readfirstlane(tid >> 6);
  const int xcd = blockIdx.x & 7, j = blockIdx.x >> 3;
  const int br = j & 1, idx = j >> 1;
  const int bg = idx & 3, qhead = 4 * xcd + (idx >> 2), rot = 4 * (((idx >> 2) + bg) & 3);
  const char* xg = (br ? xB : xA) + (size_t)bg * 128 * 2048;
  const char* wq = (br ? wqB : wqA) + qhead * 256;
  const int c = l & 15, g = l >> 4, q4 = c >> 2, p = l & 3;
  const int u = w & 3, dh = w >> 2;
  const int b = br ? 4 * bg + u : (xcd & 1) * 8 + (idx >> 1);
  const int head = br ? qhead : 8 * (xcd >> 1) + 4 * (idx & 1) + u;
  const unsigned ring = (unsigned)((w >= 4 ? w - 4 : w < 2 ? w + 4 : w + 6) * 16384);
  STAMP_DECL;
  STAMP(0);
  if (ASLEEP && !br) {
#pragma unroll 1
    for (int i = 0; i < ASLEEP; ++i) __builtin_amdgcn_s_sleep(16);
  }
  const size_t kvoff = ((size_t)(br ? b * 32 + head : b * 4 + (xcd >> 1)) * 512 + dh * 256) * 256;
  const char* kg = (br ? kB : kA) + kvoff;
  const char* vg = (br ? vB : vA) + kvoff;
  unsigned vk[4], vv[2];
#pragma unroll
  for (int i = 0; i < 4; ++i) vk[i] = (unsigned)(g * 256 + ((c ^ g ^ (4 * i)) * 16));
#pragma unroll
  for (int i = 0; i < 2; ++i) vv[i] = (unsigned)(g * 256 + ((c ^ (2 * g) ^ (8 * i)) * 16));
  auto slot_off = [](int t) constexpr { return t * 4096; };
  auto tile_to = [&](int blk, int isV, int t, unsigned base) {
    const unsigned la = lbase + base + t * 4096;
    const char* src = (isV ? vg : kg) + (blk * 4 + t) * 4096;
    if (isV) glds4<true>(src, vv[0], vv[1], vv[0], vv[1], la);
    else glds4<true>(src, vk[0], vk[1], vk[2], vk[3], la);
  };
  auto tile_issue = [&](int blk, int isV, int t) { tile_to(blk, isV, t, ring); };

  unsigned* ctl = (unsigned*)(ows + CNT_OFF);
  const __amdgpu_buffer_rsrc_t rc = __builtin_amdgcn_make_buffer_rsrc(ows + CNT_OFF, 0, 65536 + 16384, 0x00020000);
  unsigned nonce = 0;
  auto echo = [&]() {
    const u32x4 r = __builtin_amdgcn_raw_buffer_load_b128(rc, l * 16, 0, 16);
    __builtin_amdgcn_raw_buffer_store_b128(r, rc, 4096 + l * 16, 0, 16);
  };
  if (w == 0) {
    nonce = __hip_atomic_load(ctl + 1024 + blockIdx.x, RLX_AGENT) + 1u;
    if (l == 0) __hip_atomic_store(ctl + blockIdx.x, nonce, RLX_AGENT);
  } else if (w == 1 && blockIdx.x == 0) {
#pragma unroll
    for (int i = 0; i < 6; ++i) __builtin_amdgcn_raw_buffer_store_b32(0u, rc, 8192 + (i * 64 + l) * 128, 0, 16);
    WAIT_V(0);
    __builtin_amdgcn_s_sleep(24);
    echo();
  }
  unsigned* cnt = ctl + 2048 + 32;
  auto confirm = [&]() {
    unsigned spins = 0;
    while (__hip_atomic_load(ctl + 1024 + blockIdx.x, RLX_AGENT) != nonce && ++spins < (1u << 18)) __builtin_amdgcn_s_sleep(2);
    if (l == 0) __hip_atomic_fetch_add(ctl + 2048, 1u, RLX_AGENT);
  };
  f32x4 qa[4][4];
  gemm_core<8192, 0, true>(smem, lbase, wq, xg, rot, w, l, qa, [&](int n) { tile_issue(0, 0, n); }, br != 0, nullptr GSPASS);
  bf16x8 qf[2][4];
  if (br) {
    if (w < 4) {
#pragma unroll
      for (int t = 0; t < 4; ++t)
#pragma unroll
        for (int v = 0; v < 4; ++v) {
          const int tau = 4 * (w & 1) + t;
          u32x2 pkd = {pk2(qa[t][v][0], qa[t][v][1]), pk2(qa[t][v][2], qa[t][v][3])};
          const unsigned off = 98304 + ((w >> 1) * 4 + v) * 4096 + (tau >> 1) * 1024 + ((2 * (tau & 1) + (g >> 1)) * 16 + c) * 16 + (g & 1) * 8;
          *(u32x2*)(smem + off) = pkd;
        }
    }
    WAIT_L0();
    BAR();
#pragma unroll
    for (int v = 0; v < 2; ++v)
#pragma unroll
      for (int s = 0; s < 4; ++s) qf[v][s] = lds_b128(smem, 98304 + u * 8192 + v * 4096 + s * 1024 + l * 16);
    if (w == 0) confirm();
    if (w < 4) {
#pragma unroll 1
      for (int t = 0; t < 4; ++t) tile_issue(0, 0, t);
    }
  } else {
    const __amdgpu_buffer_rsrc_t rq = __builtin_amdgcn_make_buffer_rsrc(ows + Q_OFF, 0, 4u << 20, 0x00020000);
    if (w < 4) {
      const int qo = ((bg * 8 + (w >> 1) * 4) * 128 + qhead * 4 + (w & 1) * 2) * 1024 + ((g >> 1) * 16 + c) * 16 + (g & 1) * 8;
#pragma unroll
      for (int t = 0; t < 4; ++t)
#pragma unroll
        for (int v = 0; v < 4; ++v) {
          u32x2 pkd = {pk2(qa[t][v][0], qa[t][v][1]), pk2(qa[t][v][2], qa[t][v][3])};
          __builtin_amdgcn_raw_buffer_store_b64(pkd, rq, qo + v * (128 * 1024) + t * 512, 0, 16);
        }
      if (w == 1 && blockIdx.x == 0) echo();
      if (w == 0) confirm();
      WAIT_V(0);
    }
    BAR();
    if (tid == 0) __hip_atomic_fetch_add(cnt + (288 + xcd * 4 + bg) * 32, 1u, RLX_AGENT);
#pragma unroll 1
    for (int ph = 0; ph < 3; ++ph) tile_to(ph >> 1, ph & 1, u, ph * 32768 + dh * 16384);
    if (w == 1 && blockIdx.x == 0) {
      unsigned spins = 0;
      while (__hip_atomic_load(ctl + 2048, RLX_AGENT) < 256u && ++spins < (1u << 17)) { echo(); __builtin_amdgcn_s_sleep(8); }
    }
    if (w == 4) poll_ge(cnt + (288 + (head >> 2) * 4 + (b >> 2)) * 32, 4u);
    BAR();
#pragma unroll
    for (int v = 0; v < 2; ++v)
#pragma unroll
      for (int s = 0; s < 4; ++s)
        qf[v][s] = __builtin_bit_cast(bf16x8, __builtin_amdgcn_raw_buffer_load_b128(rq, ((2 * b + v) * 128 + head * 4 + s) * 1024 + l * 16, 0, 16));
    WAIT_V(0);
    tile_to(1, 1, u, 3 * 32768 + dh * 16384);
  }
  STAMP(1);
#define STAMPA(k) STAMP(k)

  unsigned ak[4], av[8];
#pragma unroll
  for (int s = 0; s < 4; ++s) ak[s] = (unsigned)(c * 256 + ((g ^ c ^ (4 * s)) * 16));
  {
    const int row = 4 * g + q4, F = 2 * (row & 7);
#pragma unroll
    for (int t = 0; t < 8; ++t) av[t] = (unsigned)(row * 256 + ((((2 * t) ^ F) + (p >> 1)) * 16) + (p & 1) * 8);
  }
  f32x4 o[8][2];
#pragma unroll
  for (int t = 0; t < 8; ++t) { o[t][0] = f32x4{0, 0, 0, 0}; o[t][1] = f32x4{0, 0, 0, 0}; }
  unsigned tb = ring, pnext = dh * 16384u, pfree = 4 * 32768u + dh * 16384u;
  float m[2] = {-__builtin_inff(), -__builtin_inff()}, ls[2] = {0.f, 0.f};
#pragma unroll 1
  for (int blk = 0; blk < 4; ++blk) {
    f32x4 sc[4][2];
    if (!br) {
      if (blk < 3) WAIT_V(12); else WAIT_V(4);
      BAR();
      if (blk < 2) tile_to(blk + 2, 0, u, pfree);
      pfree = tb; tb = pnext; pnext = pnext >= 4 * 32768u ? pnext - 4 * 32768u : pnext + 32768u;
    }
#pragma unroll
    for (int t = 0; t < 4; ++t) {
      if (br) WAIT_V(12);
      bf16x8 kf[4];
#pragma unroll
      for (int s = 0; s < 4; ++s) kf[s] = lds_b128(smem, ak[s] + tb + slot_off(t));
      WAIT_L0();
      if (br) tile_issue(blk, 1, t);
      sc[t][0] = f32x4{0, 0, 0, 0}; sc[t][1] = f32x4{0, 0, 0, 0};
#pragma unroll
      for (int s = 0; s < 4; ++s)
#pragma unroll
        for (int v = 0; v < 2; ++v) sc[t][v] = mfma16(kf[s], qf[v][s], sc[t][v]);
      SB();
    }
    bf16x8 pf[2][2];
#pragma unroll
    for (int v = 0; v < 2; ++v) {
      float mx = sc[0][v][0];
#pragma unroll
      for (int t = 0; t < 4; ++t)
#pragma unroll
        for (int i = 0; i < 4; ++i) mx = fmaxf(mx, sc[t][v][i]);
      mx = xg_max(mx) * SL2E;
      const float mn = fmaxf(m[v], mx);
      const float alpha = __builtin_amdgcn_exp2f(m[v] - mn);
      ls[v] *= alpha;
#pragma unroll
      for (int t = 0; t < 8; ++t) o[t][v] *= alpha;
      m[v] = mn;
      float sum = 0.f;
#pragma unroll
      for (int t = 0; t < 4; ++t)
#pragma unroll
        for (int i = 0; i < 4; ++i) { const float e = __builtin_amdgcn_exp2f(fmaf(sc[t][v][i], SL2E, -mn)); sc[t][v][i] = e; sum += e; }
      ls[v] += sum;
#pragma unroll
      for (int kb = 0; kb < 2; ++kb) {
        u32x4 pw = {pk2(sc[2 * kb][v][0], sc[2 * kb][v][1]), pk2(sc[2 * kb][v][2], sc[2 * kb][v][3]),
                    pk2(sc[2 * kb + 1][v][0], sc[2 * kb + 1][v][1]), pk2(sc[2 * kb + 1][v][2], sc[2 * kb + 1][v][3])};
        pf[kb][v] = __builtin_bit_cast(bf16x8, pw);
      }
    }
    const bool more = blk < 3;
    if (!br) {
      if (blk < 2) WAIT_V(12); else if (blk == 2) WAIT_V(8); else WAIT_V(0);
      BAR();
      pfree = tb;
      if (blk < 2) tile_to(blk + 2, 1, u, pfree);
      tb = pnext; pnext = pnext >= 4 * 32768u ? pnext - 4 * 32768u : pnext + 32768u;
      pfree = tb;
    }
#pragma unroll
    for (int kb = 0; kb < 2; ++kb) {
      s16x4 vlo[8], vhi[8];
      if (br) { if (kb == 0 || more) WAIT_V(12); else WAIT_V(4); }
#pragma unroll
      for (int t = 0; t < 8; ++t) vlo[t] = lds_tr(smem, av[t] + tb + slot_off(2 * kb));
      if (br) {
        WAIT_L0();
        if (more) { tile_issue(blk + 1, 0, 2 * kb); WAIT_V(12); }
        else if (kb == 0) WAIT_V(8);
        else WAIT_V(0);
      }
#pragma unroll
      for (int t = 0; t < 8; ++t) vhi[t] = lds_tr(smem, av[t] + tb + slot_off(2 * kb + 1));
      WAIT_L0();
      if (br && more) tile_issue(blk + 1, 0, 2 * kb + 1);
#pragma unroll
      for (int t = 0; t < 8; ++t)
#pragma unroll
        for (int v = 0; v < 2; ++v) o[t][v] = mfma16(cat8(vlo[t], vhi[t]), pf[kb][v], o[t][v]);
      SB();
    }
    STAMPA(2 + blk);
  }
  if (!br) BAR();
  ls[0] = xg_sum(ls[0]);
  ls[1] = xg_sum(ls[1]);
  if (dh) {
#pragma unroll
    for (int t = 0; t < 8; ++t) { const f32x4 tmp = o[t][0]; o[t][0] = o[t][1]; o[t][1] = tmp; }
    float tm = m[0]; m[0] = m[1]; m[1] = tm;
    tm = ls[0]; ls[0] = ls[1]; ls[1] = tm;
  }
  {
#pragma unroll
    for (int t = 0; t < 8; ++t) *(f32x4*)(smem + ring + t * 1024 + l * 16) = o[t][1];
    f32x2 ml = {m[1], ls[1]};
    *(f32x2*)(smem + ring + 8192 + l * 8) = ml;
  }
  WAIT_L0();
  BAR();
  {
    const int pw = w ^ 4;
    const unsigned pring = (unsigned)((pw >= 4 ? pw - 4 : pw < 2 ? pw + 4 : pw + 6) * 16384);
    const f32x2 ml = *(const f32x2*)(smem + pring + 8192 + l * 8);
    const float mo = m[0], lo = ls[0];
    const float mm = fmaxf(mo, ml[0]);
    float a0 = __builtin_amdgcn_exp2f(mo - mm), a1 = __builtin_amdgcn_exp2f(ml[0] - mm);
    const float inv = 1.0f / (lo * a0 + ml[1] * a1);
    a0 *= inv; a1 *= inv;
    const __amdgpu_buffer_rsrc_t ro = __builtin_amdgcn_make_buffer_rsrc(ows + (size_t)br * (4u << 20) + ((size_t)(2 * b + dh) * 128 + head * 4) * 1024, 0, 4096, 0x00020000);
#pragma unroll
    for (int t = 0; t < 8; ++t) {
      const f32x4 oth = *(const f32x4*)(smem + pring + t * 1024 + l * 16);
      const f32x4 r = o[t][0] * a0 + oth * a1;
      u32x2 pkd = {pk2(r[0], r[1]), pk2(r[2], r[3])};
      __builtin_amdgcn_raw_buffer_store_b64(pkd, ro, t * 512 + ((g >> 1) * 16 + c) * 16 + (g & 1) * 8, 0, 16);
    }
  }
  WAIT_V(0);
  WAIT_L0();
  BAR();
  if (tid == 0) __hip_atomic_fetch_add(cnt + ((br * 4 + (b >> 2)) * 4 + (head >> 3)) * 32, 1u, RLX_AGENT);
  STAMPA(6);

  {
    const int kq = xcd >> 1, mt = idx & 3, nl = idx >> 2, nt = (xcd & 1) * 4 + nl;
    if (WOPF && !br && w < 4) {
      unsigned vp[8];
#pragma unroll
      for (int i = 0; i < 8; ++i) vp[i] = l * 16 + i * 1024;
      const char* src = woB + (size_t)(kq * 1024 + idx * 64 + w * 16) * 2048 + (xcd & 1) * 1024 + 4096;
      glds8<2>(src, vp, lbase + 98304 + w * 16384 + 4096);
      glds8<2>(src + 8 * 2048, vp, lbase + 98304 + w * 16384 + 8192 + 4096);
      WAIT_V(0);
    }
    const char* og = ows + (size_t)br * (4u << 20) + ((size_t)(mt * 8) * 128 + kq * 32) * 1024;
    const char* wo = (br ? woB : woA) + (size_t)(kq * 1024) * 2048 + nt * 256;
    f32x4 acc[4][4];
    gemm_core<2048, 1, false>(smem, lbase, wo, og, 4 * ((mt + nl) & 3), w, l, acc, [](int) {}, false, cnt + ((br * 4 + mt) * 4 + kq) * 32 GSPASS2);
    STAMPA(7);
    const int tile = br * 32 + nt * 4 + mt;
    const __amdgpu_buffer_rsrc_t rs = __builtin_amdgcn_make_buffer_rsrc(ows + PART_OFF + (size_t)tile * (16 * 16384), 0, 16 * 16384, 0x00020000);
    unsigned* rcnt = cnt + (32 + tile * 4) * 32;
    if (w < 4) {
      if (w != kq) {
        const int off = (w * 4 + kq) * 16384 + l * 16;
#pragma unroll
        for (int t = 0; t < 4; ++t)
#pragma unroll
          for (int v = 0; v < 4; ++v) __builtin_amdgcn_raw_buffer_store_b128(__builtin_bit_cast(u32x4, acc[t][v]), rs, off + (t * 4 + v) * 1024, 0, 16);
        WAIT_V(0);
        if (l == 0) __hip_atomic_fetch_add(rcnt + w * 32, 1u, RLX_AGENT);
      } else {
#pragma unroll
        for (int t = 0; t < 4; ++t)
#pragma unroll
          for (int v = 0; v < 4; ++v) *(f32x4*)(smem + (t * 4 + v) * 1024 + l * 16) = acc[t][v];
        WAIT_L0();
      }
    } else if (w == 4) {
      poll_ge(rcnt + kq * 32, 3u);
    }
    asm volatile("" ::: "memory");
    BAR();
    asm volatile("" ::: "memory");
    u32x4 pr[3][2];
#pragma unroll
    for (int s = 0; s < 3; ++s) {
      const int src = s + (s >= kq);
#pragma unroll
      for (int i = 0; i < 2; ++i) pr[s][i] = __builtin_amdgcn_raw_buffer_load_b128(rs, (kq * 4 + src) * 16384 + (2 * w + i) * 1024 + l * 16, 0, 16);
    }
#pragma unroll
    for (int i = 0; i < 2; ++i) {
      const f32x4 own = *(const f32x4*)(smem + (2 * w + i) * 1024 + l * 16);
      const f32x4 r = (__builtin_bit_cast(f32x4, pr[0][i]) + __builtin_bit_cast(f32x4, pr[1][i])) + (__builtin_bit_cast(f32x4, pr[2][i]) + own);
      const int mm = mt * 128 + (kq >> 1) * 64 + ((w & 1) * 2 + i) * 16 + c, nn = nt * 128 + (kq & 1) * 64 + (w >> 1) * 16 + g * 4;
      u32x2 pkd = {pk2(r[0], r[1]), pk2(r[2], r[3])};
      *(u32x2*)(out + ((size_t)mm * 2048 + br * 1024 + nn) * 2) = pkd;
    }
  }
  STAMPA(8);
  STAMP_FLUSH(ows);
}

extern "C" void kernel_launch(void* const* d_in, const int* in_sizes, int n_in, void* d_out, int out_size, void* d_ws, size_t ws_size,
                              hipStream_t stream) {
  k1_qproj_attn<<<256, 512, 0, stream>>>((const char*)d_in[0], (const char*)d_in[1], (const char*)d_in[2], (const char*)d_in[3], (const char*)d_in[5],
                                         (const char*)d_in[6], (const char*)d_in[7], (const char*)d_in[8], (char*)d_ws, (const char*)d_in[4],
                                         (const char*)d_in[9], (char*)d_out);
}
```

```cpp
#include <hip/hip_runtime.h>
#include <hip/hip_bf16.h>

#define POL_Q 2
#define POL_KVA 2

typedef short bf16x8 __attribute__((ext_vector_type(8)));
typedef short s16x4 __attribute__((ext_vector_type(4)));
typedef float f32x4 __attribute__((ext_vector_type(4)));
typedef float f32x2 __attribute__((ext_vector_type(2)));
typedef __bf16 bf16x2 __attribute__((ext_vector_type(2)));
typedef unsigned u32x4 __attribute__((ext_vector_type(4)));
typedef unsigned u32x2 __attribute__((ext_vector_type(2)));
#define LAS __attribute__((address_space(3)))
#define DI __device__ __forceinline__

#define WAIT_V(n) asm volatile("s_waitcnt vmcnt(%0)" ::"n"(n) : "memory")
#define WAIT_L0() do { asm volatile("" ::: "memory"); __builtin_amdgcn_s_waitcnt(0xC07F); asm volatile("" ::: "memory"); } while (0)
#define BAR() __builtin_amdgcn_s_barrier()
#define SB() __builtin_amdgcn_sched_barrier(0)

template <bool NT>
DI void glds(const char* sbase, unsigned voff, unsigned ldsaddr) {
  if (NT)
    asm volatile("s_mov_b32 m0, %2\n\ts_nop 0\n\tglobal_load_lds_dwordx4 %0, %1 nt" ::"v"(voff), "s"(sbase), "s"(ldsaddr) : "memory");
  else
    asm volatile("s_mov_b32 m0, %2\n\ts_nop 0\n\tglobal_load_lds_dwordx4 %0, %1" ::"v"(voff), "s"(sbase), "s"(ldsaddr) : "memory");
}
template <bool NT>
DI void glds4(const char* sbase, unsigned v0, unsigned v1, unsigned v2, unsigned v3, unsigned ldsaddr) {
  if (NT)
    asm volatile("s_mov_b32 m0, %5\n\ts_nop 0\n\t"
                 "global_load_lds_dwordx4 %0, %4 nt\n\t"
                 "global_load_lds_dwordx4 %1, %4 offset:1024 nt\n\t"
                 "global_load_lds_dwordx4 %2, %4 offset:2048 nt\n\t"
                 "global_load_lds_dwordx4 %3, %4 offset:3072 nt"
                 ::"v"(v0), "v"(v1), "v"(v2), "v"(v3), "s"(sbase), "s"(ldsaddr) : "memory");
  else
    asm volatile("s_mov_b32 m0, %5\n\ts_nop 0\n\t"
                 "global_load_lds_dwordx4 %0, %4\n\t"
                 "global_load_lds_dwordx4 %1, %4 offset:1024\n\t"
                 "global_load_lds_dwordx4 %2, %4 offset:2048\n\t"
                 "global_load_lds_dwordx4 %3, %4 offset:3072"
                 ::"v"(v0), "v"(v1), "v"(v2), "v"(v3), "s"(sbase), "s"(ldsaddr) : "memory");
}
DI void pin(const char* sbase, unsigned voff, unsigned ldsaddr) {
  asm volatile("s_mov_b32 m0, %2\n\ts_nop 0\n\tglobal_load_lds_dword %0, %1" ::"v"(voff), "s"(sbase), "s"(ldsaddr) : "memory");
}

DI f32x4 mfma16(bf16x8 a, bf16x8 b, f32x4 c) { return __builtin_amdgcn_mfma_f32_16x16x32_bf16(a, b, c, 0, 0, 0); }
DI unsigned pk2(float lo, float hi) { f32x2 a = {lo, hi}; return __builtin_bit_cast(unsigned, __builtin_convertvector(a, bf16x2)); }
DI bf16x8 lds_b128(const char* smem, unsigned off) { return *(const bf16x8*)(smem + off); }
DI s16x4 lds_tr(char* smem, unsigned off) { return __builtin_amdgcn_ds_read_tr16_b64_v4i16((LAS s16x4*)(smem + off)); }
DI bf16x8 cat8(s16x4 lo, s16x4 hi) { return __builtin_shufflevector(lo, hi, 0, 1, 2, 3, 4, 5, 6, 7); }
DI float xg_max(float x) {
  auto s = __builtin_amdgcn_permlane16_swap(__float_as_uint(x), __float_as_uint(x), false, false);
  x = fmaxf(__uint_as_float(s[0]), __uint_as_float(s[1]));
  auto t = __builtin_amdgcn_permlane32_swap(__float_as_uint(x), __float_as_uint(x), false, false);
  return fmaxf(__uint_as_float(t[0]), __uint_as_float(t[1]));
}
DI float xg_sum(float x) {
  auto s = __builtin_amdgcn_permlane16_swap(__float_as_uint(x), __float_as_uint(x), false, false);
  x = __uint_as_float(s[0]) + __uint_as_float(s[1]);
  auto t = __builtin_amdgcn_permlane32_swap(__float_as_uint(x), __float_as_uint(x), false, false);
  return __uint_as_float(t[0]) + __uint_as_float(t[1]);
}

#define STAMP(k)
#define STAMP_DECL
#define STAMP_FLUSH(ws)
constexpr float SL2E = 0.088388347648318447f * 1.4426950408889634f;

constexpr size_t PART_OFF = 16u << 20, CNT_OFF = 8u << 20, Q_OFF = 10u << 20;
#define ASLEEP 0

#define G8_(j, off, sfx) "global_load_lds_dwordx4 %" #j ", %8 offset:" #off sfx "\n\t"
template <int MODE>
DI void glds8(const char* sbase, const unsigned (&v)[8], unsigned ldsaddr) {
  if (MODE == 0)
    asm volatile("s_mov_b32 m0, %9\n\ts_nop 0\n\t" G8_(0, -4096, "") G8_(1, -3072, "") G8_(2, -2048, "") G8_(3, -1024, "") G8_(4, 0, "") G8_(5, 1024, "") G8_(6, 2048, "") G8_(7, 3072, "")
                 ::"v"(v[0]), "v"(v[1]), "v"(v[2]), "v"(v[3]), "v"(v[4]), "v"(v[5]), "v"(v[6]), "v"(v[7]), "s"(sbase), "s"(ldsaddr) : "memory");
  else if (MODE == 3)
    asm volatile("s_mov_b32 m0, %9\n\ts_nop 0\n\t" G8_(0, -4096, " sc1") G8_(1, -3072, " sc1") G8_(2, -2048, " sc1") G8_(3, -1024, " sc1") G8_(4, 0, " sc1") G8_(5, 1024, " sc1") G8_(6, 2048, " sc1") G8_(7, 3072, " sc1")
                 ::"v"(v[0]), "v"(v[1]), "v"(v[2]), "v"(v[3]), "v"(v[4]), "v"(v[5]), "v"(v[6]), "v"(v[7]), "s"(sbase), "s"(ldsaddr) : "memory");
  else if (MODE == 1)
    asm volatile("s_mov_b32 m0, %9\n\ts_nop 0\n\t" G8_(0, -4096, " nt") G8_(1, -3072, " nt") G8_(2, -2048, " nt") G8_(3, -1024, " nt") G8_(4, 0, " nt") G8_(5, 1024, " nt") G8_(6, 2048, " nt") G8_(7, 3072, " nt")
                 ::"v"(v[0]), "v"(v[1]), "v"(v[2]), "v"(v[3]), "v"(v[4]), "v"(v[5]), "v"(v[6]), "v"(v[7]), "s"(sbase), "s"(ldsaddr) : "memory");
  else
    asm volatile("s_mov_b32 m0, %9\n\ts_nop 0\n\t" G8_(0, -4096, " nt") G8_(0, -4096, "") G8_(1, -3072, " nt") G8_(1, -3072, "") G8_(2, -2048, " nt") G8_(2, -2048, "") G8_(3, -1024, " nt") G8_(3, -1024, "")
                 G8_(4, 0, " nt") G8_(4, 0, "") G8_(5, 1024, " nt") G8_(5, 1024, "") G8_(6, 2048, " nt") G8_(6, 2048, "") G8_(7, 3072, " nt") G8_(7, 3072, "")
                 ::"v"(v[0]), "v"(v[1]), "v"(v[2]), "v"(v[3]), "v"(v[4]), "v"(v[5]), "v"(v[6]), "v"(v[7]), "s"(sbase), "s"(ldsaddr) : "memory");
}
#define CVAR 1
#define RPG 2
#define H0SLEEP 12
#define KVAPOL 0
#define BLKBAR 0
#define BLKBAR2 0
#define BNT 1
#define FNT 1
#define WOPF 1
#define NT_VB 1
#define NT_KB 1
#define NT_A 1
#define FDUP 1
#define GSARG
#define GSPASS
#define GSPASS2
#define GBAR() BAR()
#define RLX_AGENT __ATOMIC_RELAXED, __HIP_MEMORY_SCOPE_AGENT
DI void poll_ge(unsigned* p, unsigned want) {
  unsigned spins = 0;
  while (__hip_atomic_load(p, RLX_AGENT) < want && ++spins < (1u << 22)) __builtin_amdgcn_s_sleep(1);
  asm volatile("" ::: "memory");
}
struct HFrags { s16x4 a[4][2]; bf16x8 b[4]; };
template <int WS, int XT, bool TAIL, class TailF>
DI void gemm_core(char* smem, unsigned lbase, const char* wg, const char* xg, int rot, int w, int l, f32x4 (&acc)[4][4], TailF tail, bool dotail, unsigned* gate GSARG) {
  int bn_ = 1; (void)bn_;
  const int c = l & 15, g = l >> 4, q4 = c >> 2, p = l & 3;
  if (w >= 4) {
    const int iw = w & 1;
    unsigned vw[8], vx[8];
    {
      const unsigned vw0 = (unsigned)(g * WS + ((c ^ (2 * g)) * 16)), vw1 = vw0 ^ 128;
      const unsigned vx0 = XT ? (unsigned)(l * 16) : (unsigned)((l >> 3) * 2048 + (((l & 7) ^ g) * 16));
      const unsigned vx1 = XT ? vx0 : (vx0 ^ 64);
#pragma unroll
      for (int j = 0; j < 8; ++j) {
        vw[j] = ((j & 2) ? vw1 : vw0) + j * (4 * WS - 1024);
        vx[j] = XT ? vx0 + (j >> 1) * (128 * 1024 - 2048) : ((j & 1) ? vx1 : vx0) + j * (16384 - 1024);
      }
    }
    const char* w_w = wg + (size_t)(32 * iw) * WS + 4096;
    const char* x_w = (XT ? xg + (size_t)(4 * iw) * (128 * 1024) : xg + (size_t)(64 * iw) * 2048) + 4096;
    const unsigned lw0 = lbase + iw * 8192 + 4096;
    auto wpieces = [&](int kap, unsigned bufoff, bool first) {
      if (first && FNT) glds8<FDUP ? 2 : 1>(w_w + (size_t)kap * (64 * WS), vw, lw0 + bufoff);
      else glds8<0>(w_w + (size_t)kap * (64 * WS), vw, lw0 + bufoff);
    };
    auto xpieces = [&](int kap, unsigned bufoff) { glds8<XT ? 3 : 0>(x_w + kap * (XT ? 2048 : 128), vx, lw0 + bufoff + 16384); };
    constexpr int FN = (FDUP && FNT) ? 24 : 16;
    if (w < 6) {
      if (XT) {
#pragma unroll 1
        for (int f = 0; f < 2; ++f) wpieces(rot + f, f * 32768, true);
        if (w == 4) poll_ge(gate, 8u);
        BAR();
#pragma unroll 1
        for (int f = 0; f < 2; ++f) xpieces(rot + f, f * 32768);
        WAIT_V(8);
      } else {
#pragma unroll 1
        for (int f = 0; f < 2; ++f) { wpieces(rot + f, f * 32768, true); xpieces(rot + f, f * 32768); }
      }
#pragma unroll 1
      for (int i = 0; i < 2; ++i) {
        WAIT_V(FN);
        GBAR();
        GBAR();
        wpieces(rot + i + 2, i * 32768, true); xpieces(rot + i + 2, i * 32768);
        GBAR();
        GBAR();
      }
      WAIT_V(FN);
      GBAR();
      GBAR();
      if (TAIL && dotail) tail(0);
      GBAR();
      if (TAIL && dotail) tail(1);
      GBAR();
      if (TAIL && dotail) { tail(2); WAIT_V(12); } else WAIT_V(0);
      GBAR();
      if (TAIL && dotail) tail(3);
      GBAR();
      GBAR();
      GBAR();
    } else {
      auto stage = [&](int kap, int hb) { wpieces(kap & 15, 65536 + hb * 32768, false); xpieces(kap & 15, 65536 + hb * 32768); };
      __builtin_amdgcn_s_sleep(H0SLEEP);
      if (XT) { wpieces((rot + 4) & 15, 65536, false); BAR(); xpieces((rot + 4) & 15, 65536); }
      else stage(rot + 4, 0);
      GBAR();
      stage(rot + 8, 1);
      WAIT_V(16);
      GBAR();
      stage(rot + 12, 2);
#pragma unroll 1
      for (int i = 0; i < 3; ++i) {
        WAIT_V(16);
        GBAR();
        stage(rot + 5 + i, 0);
        WAIT_V(16);
        GBAR();
        stage(rot + 9 + i, 1);
        GBAR();
        stage(rot + 13 + i, 2);
        WAIT_V(32);
        GBAR();
      }
      if (TAIL && dotail) { tail(0); tail(1); WAIT_V(24); } else WAIT_V(16);
      GBAR();
      if (TAIL && dotail) { tail(2); tail(3); WAIT_V(16); } else WAIT_V(0);
      GBAR();
    }
  } else {
    if (XT) BAR();
    const int nh = w & 1, mh = w >> 1;
    unsigned aw[4];
    {
      const int F = 2 * q4 | 8 * (g & 1);
#pragma unroll
      for (int t = 0; t < 4; ++t) aw[t] = (unsigned)((8 * g + q4) * 256 + ((((2 * (4 * nh + t)) ^ F) + (p >> 1)) * 16) + (p & 1) * 8);
    }
    const unsigned ax0 = XT ? (unsigned)(16384 + mh * 8192 + l * 16) : (unsigned)(16384 + (64 * mh + c) * 128 + ((g ^ (c >> 1)) * 16));
    const unsigned ax1 = XT ? ax0 + 1024 : (ax0 ^ 64);
    struct FF { HFrags h[2]; };
    auto mm = [&](const FF& fo) {
#pragma unroll
      for (int ks = 0; ks < 2; ++ks)
#pragma unroll
        for (int t = 0; t < 4; ++t)
#pragma unroll
          for (int v = 0; v < 4; ++v) acc[t][v] = mfma16(cat8(fo.h[ks].a[t][0], fo.h[ks].a[t][1]), fo.h[ks].b[v], acc[t][v]);
    };
    auto full = [&](FF& fn, const FF& fo, unsigned bo) {
#pragma unroll
      for (int ks = 0; ks < 2; ++ks)
#pragma unroll
        for (int t = 0; t < 4; ++t) {
          fn.h[ks].b[t] = lds_b128(smem, (ks ? ax1 : ax0) + bo + t * 2048);
          fn.h[ks].a[t][0] = lds_tr(smem, aw[t] + bo + ks * 8192);
          fn.h[ks].a[t][1] = lds_tr(smem, aw[t] + bo + ks * 8192 + 1024);
        }
      mm(fo);
#pragma unroll
      for (int t = 0; t < 24; ++t) {
        __builtin_amdgcn_sched_group_barrier(0x008, 1, 0);
        __builtin_amdgcn_sched_group_barrier(0x100, 1, 0);
      }
      __builtin_amdgcn_sched_group_barrier(0x008, 8, 0);
      SB();
    };
#pragma unroll
    for (int t = 0; t < 4; ++t)
#pragma unroll
      for (int v = 0; v < 4; ++v) acc[t][v] = f32x4{0, 0, 0, 0};
    FF f0, f1;
#pragma unroll
    for (int ks = 0; ks < 2; ++ks)
#pragma unroll
      for (int t = 0; t < 4; ++t) { f1.h[ks].b[t] = bf16x8{0, 0, 0, 0, 0, 0, 0, 0}; f1.h[ks].a[t][0] = s16x4{0, 0, 0, 0}; f1.h[ks].a[t][1] = s16x4{0, 0, 0, 0}; }
#pragma unroll 1
    for (int n = 0; n < 16; n += 2) {
      const unsigned bo0 = (n & 2) ? 98304 : ((n >> 2) & 1) * 32768, bo1 = (n & 2) ? 131072 : 65536;
      WAIT_L0();
      GBAR();
      full(f0, f1, bo0);
      WAIT_L0();
      GBAR();
      full(f1, f0, bo1);
    }
    WAIT_L0();
    mm(f1);
  }
}

__global__ __launch_bounds__(512, 2) void k1_qproj_attn(const char* __restrict__ xA, const char* __restrict__ kA, const char* __restrict__ vA,
                                                        const char* __restrict__ wqA, const char* __restrict__ xB, const char* __restrict__ kB,
                                                        const char* __restrict__ vB, const char* __restrict__ wqB, char* __restrict__ ows,
                                                        const char* __restrict__ woA, const char* __restrict__ woB, char* __restrict__ out) {
  __shared__ __attribute__((aligned(1024))) char smem[163840];
  const unsigned lbase = (unsigned)(size_t)(LAS char*)smem;
  const int tid = threadIdx.x, l = tid & 63, w = __builtin_amdgcn_readfirstlane(tid >> 6);
  const int xcd = blockIdx.x & 7, j = blockIdx.x >> 3;
  const int br = j & 1, idx = j >> 1;
  const int bg = idx & 3, qhead = 4 * xcd + (idx >> 2), rot = 4 * (((idx >> 2) + bg) & 3);
  const char* xg = (br ? xB : xA) + (size_t)bg * 128 * 2048;
  const char* wq = (br ? wqB : wqA) + qhead * 256;
  const int c = l & 15, g = l >> 4, q4 = c >> 2, p = l & 3;
  const int u = w & 3, dh = w >> 2;
  const int b = br ? 4 * bg + u : (xcd & 1) * 8 + (idx >> 1);
  const int head = br ? qhead : 8 * (xcd >> 1) + 4 * (idx & 1) + u;
  const unsigned ring = (unsigned)((w >= 4 ? w - 4 : w < 2 ? w + 4 : w + 6) * 16384);
  STAMP_DECL;
  STAMP(0);
  if (ASLEEP && !br) {
#pragma unroll 1
    for (int i = 0; i < ASLEEP; ++i) __builtin_amdgcn_s_sleep(16);
  }
  const size_t kvoff = ((size_t)(br ? b * 32 + head : b * 4 + (xcd >> 1)) * 512 + dh * 256) * 256;
  const char* kg = (br ? kB : kA) + kvoff;
  const char* vg = (br ? vB : vA) + kvoff;
  unsigned vk[4], vv[2];
#pragma unroll
  for (int i = 0; i < 4; ++i) vk[i] = (unsigned)(g * 256 + ((c ^ g ^ (4 * i)) * 16));
#pragma unroll
  for (int i = 0; i < 2; ++i) vv[i] = (unsigned)(g * 256 + ((c ^ (2 * g) ^ (8 * i)) * 16));
  auto slot_off = [](int t) constexpr { return t * 4096; };
  auto tile_to = [&](int blk, int isV, int t, unsigned base) {
    const unsigned la = lbase + base + t * 4096;
    const char* src = (isV ? vg : kg) + (blk * 4 + t) * 4096;
    const bool nt = br ? (isV ? NT_VB : NT_KB) : NT_A;
    if (nt) {
      if (isV) glds4<true>(src, vv[0], vv[1], vv[0], vv[1], la);
      else glds4<true>(src, vk[0], vk[1], vk[2], vk[3], la);
    } else {
      if (isV) glds4<false>(src, vv[0], vv[1], vv[0], vv[1], la);
      else glds4<false>(src, vk[0], vk[1], vk[2], vk[3], la);
    }
  };
  auto tile_issue = [&](int blk, int isV, int t) { tile_to(blk, isV, t, ring); };

  unsigned* ctl = (unsigned*)(ows + CNT_OFF);
  const __amdgpu_buffer_rsrc_t rc = __builtin_amdgcn_make_buffer_rsrc(ows + CNT_OFF, 0, 65536 + 16384, 0x00020000);
  unsigned nonce = 0;
  auto echo = [&]() {
    const u32x4 r = __builtin_amdgcn_raw_buffer_load_b128(rc, l * 16, 0, 16);
    __builtin_amdgcn_raw_buffer_store_b128(r, rc, 4096 + l * 16, 0, 16);
  };
  if (w == 0) {
    nonce = __hip_atomic_load(ctl + 1024 + blockIdx.x, RLX_AGENT) + 1u;
    if (l == 0) __hip_atomic_store(ctl + blockIdx.x, nonce, RLX_AGENT);
  } else if (w == 1 && blockIdx.x == 0) {
#pragma unroll
    for (int i = 0; i < 6; ++i) __builtin_amdgcn_raw_buffer_store_b32(0u, rc, 8192 + (i * 64 + l) * 128, 0, 16);
    WAIT_V(0);
    __builtin_amdgcn_s_sleep(24);
    echo();
  }
  unsigned* cnt = ctl + 2048 + 32;
  auto confirm = [&]() {
    unsigned spins = 0;
    while (__hip_atomic_load(ctl + 1024 + blockIdx.x, RLX_AGENT) != nonce && ++spins < (1u << 18)) __builtin_amdgcn_s_sleep(2);
    if (l == 0) __hip_atomic_fetch_add(ctl + 2048, 1u, RLX_AGENT);
  };
  f32x4 qa[4][4];
  gemm_core<8192, 0, true>(smem, lbase, wq, xg, rot, w, l, qa, [&](int n) { tile_issue(0, 0, n); }, br != 0, nullptr GSPASS);
  bf16x8 qf[2][4];
  if (br) {
    if (w < 4) {
#pragma unroll
      for (int t = 0; t < 4; ++t)
#pragma unroll
        for (int v = 0; v < 4; ++v) {
          const int tau = 4 * (w & 1) + t;
          u32x2 pkd = {pk2(qa[t][v][0], qa[t][v][1]), pk2(qa[t][v][2], qa[t][v][3])};
          const unsigned off = 98304 + ((w >> 1) * 4 + v) * 4096 + (tau >> 1) * 1024 + ((2 * (tau & 1) + (g >> 1)) * 16 + c) * 16 + (g & 1) * 8;
          *(u32x2*)(smem + off) = pkd;
        }
    }
    WAIT_L0();
    BAR();
#pragma unroll
    for (int v = 0; v < 2; ++v)
#pragma unroll
      for (int s = 0; s < 4; ++s) qf[v][s] = lds_b128(smem, 98304 + u * 8192 + v * 4096 + s * 1024 + l * 16);
    if (w == 0) confirm();
    if (w < 4) {
#pragma unroll 1
      for (int t = 0; t < 4; ++t) tile_issue(0, 0, t);
    }
  } else {
    const __amdgpu_buffer_rsrc_t rq = __builtin_amdgcn_make_buffer_rsrc(ows + Q_OFF, 0, 4u << 20, 0x00020000);
    if (w < 4) {
      const int qo = ((bg * 8 + (w >> 1) * 4) * 128 + qhead * 4 + (w & 1) * 2) * 1024 + ((g >> 1) * 16 + c) * 16 + (g & 1) * 8;
#pragma unroll
      for (int t = 0; t < 4; ++t)
#pragma unroll
        for (int v = 0; v < 4; ++v) {
          u32x2 pkd = {pk2(qa[t][v][0], qa[t][v][1]), pk2(qa[t][v][2], qa[t][v][3])};
          __builtin_amdgcn_raw_buffer_store_b64(pkd, rq, qo + v * (128 * 1024) + t * 512, 0, 16);
        }
      if (w == 1 && blockIdx.x == 0) echo();
      if (w == 0) confirm();
      WAIT_V(0);
    }
    BAR();
    if (tid == 0) __hip_atomic_fetch_add(cnt + (288 + xcd * 4 + bg) * 32, 1u, RLX_AGENT);
#pragma unroll 1
    for (int ph = 0; ph < 3; ++ph) tile_to(ph >> 1, ph & 1, u, ph * 32768 + dh * 16384);
    if (w == 1 && blockIdx.x == 0) {
      unsigned spins = 0;
      while (__hip_atomic_load(ctl + 2048, RLX_AGENT) < 256u && ++spins < (1u << 17)) { echo(); __builtin_amdgcn_s_sleep(8); }
    }
    if (w == 4) poll_ge(cnt + (288 + (head >> 2) * 4 + (b >> 2)) * 32, 4u);
    BAR();
#pragma unroll
    for (int v = 0; v < 2; ++v)
#pragma unroll
      for (int s = 0; s < 4; ++s)
        qf[v][s] = __builtin_bit_cast(bf16x8, __builtin_amdgcn_raw_buffer_load_b128(rq, ((2 * b + v) * 128 + head * 4 + s) * 1024 + l * 16, 0, 16));
    WAIT_V(0);
    tile_to(1, 1, u, 3 * 32768 + dh * 16384);
  }
  STAMP(1);
#define STAMPA(k) STAMP(k)

  unsigned ak[4], av[8];
#pragma unroll
  for (int s = 0; s < 4; ++s) ak[s] = (unsigned)(c * 256 + ((g ^ c ^ (4 * s)) * 16));
  {
    const int row = 4 * g + q4, F = 2 * (row & 7);
#pragma unroll
    for (int t = 0; t < 8; ++t) av[t] = (unsigned)(row * 256 + ((((2 * t) ^ F) + (p >> 1)) * 16) + (p & 1) * 8);
  }
  f32x4 o[8][2];
#pragma unroll
  for (int t = 0; t < 8; ++t) { o[t][0] = f32x4{0, 0, 0, 0}; o[t][1] = f32x4{0, 0, 0, 0}; }
  unsigned tb = ring, pnext = dh * 16384u, pfree = 4 * 32768u + dh * 16384u;
  float m[2] = {-__builtin_inff(), -__builtin_inff()}, ls[2] = {0.f, 0.f};
#pragma unroll 1
  for (int blk = 0; blk < 4; ++blk) {
    f32x4 sc[4][2];
    if (!br) {
      if (blk < 3) WAIT_V(12); else WAIT_V(4);
      BAR();
      if (blk < 2) tile_to(blk + 2, 0, u, pfree);
      pfree = tb; tb = pnext; pnext = pnext >= 4 * 32768u ? pnext - 4 * 32768u : pnext + 32768u;
    }
#pragma unroll
    for (int t = 0; t < 4; ++t) {
      if (br) WAIT_V(12);
      bf16x8 kf[4];
#pragma unroll
      for (int s = 0; s < 4; ++s) kf[s] = lds_b128(smem, ak[s] + tb + slot_off(t));
      WAIT_L0();
      if (br) tile_issue(blk, 1, t);
      sc[t][0] = f32x4{0, 0, 0, 0}; sc[t][1] = f32x4{0, 0, 0, 0};
#pragma unroll
      for (int s = 0; s < 4; ++s)
#pragma unroll
        for (int v = 0; v < 2; ++v) sc[t][v] = mfma16(kf[s], qf[v][s], sc[t][v]);
      SB();
    }
    bf16x8 pf[2][2];
#pragma unroll
    for (int v = 0; v < 2; ++v) {
      float mx = sc[0][v][0];
#pragma unroll
      for (int t = 0; t < 4; ++t)
#pragma unroll
        for (int i = 0; i < 4; ++i) mx = fmaxf(mx, sc[t][v][i]);
      mx = xg_max(mx) * SL2E;
      const float mn = fmaxf(m[v], mx);
      const float alpha = __builtin_amdgcn_exp2f(m[v] - mn);
      ls[v] *= alpha;
#pragma unroll
      for (int t = 0; t < 8; ++t) o[t][v] *= alpha;
      m[v] = mn;
      float sum = 0.f;
#pragma unroll
      for (int t = 0; t < 4; ++t)
#pragma unroll
        for (int i = 0; i < 4; ++i) { const float e = __builtin_amdgcn_exp2f(fmaf(sc[t][v][i], SL2E, -mn)); sc[t][v][i] = e; sum += e; }
      ls[v] += sum;
#pragma unroll
      for (int kb = 0; kb < 2; ++kb) {
        u32x4 pw = {pk2(sc[2 * kb][v][0], sc[2 * kb][v][1]), pk2(sc[2 * kb][v][2], sc[2 * kb][v][3]),
                    pk2(sc[2 * kb + 1][v][0], sc[2 * kb + 1][v][1]), pk2(sc[2 * kb + 1][v][2], sc[2 * kb + 1][v][3])};
        pf[kb][v] = __builtin_bit_cast(bf16x8, pw);
      }
    }
    const bool more = blk < 3;
    if (!br) {
      if (blk < 2) WAIT_V(12); else if (blk == 2) WAIT_V(8); else WAIT_V(0);
      BAR();
      pfree = tb;
      if (blk < 2) tile_to(blk + 2, 1, u, pfree);
      tb = pnext; pnext = pnext >= 4 * 32768u ? pnext - 4 * 32768u : pnext + 32768u;
      pfree = tb;
    }
#pragma unroll
    for (int kb = 0; kb < 2; ++kb) {
      s16x4 vlo[8], vhi[8];
      if (br) { if (kb == 0 || more) WAIT_V(12); else WAIT_V(4); }
#pragma unroll
      for (int t = 0; t < 8; ++t) vlo[t] = lds_tr(smem, av[t] + tb + slot_off(2 * kb));
      if (br) {
        WAIT_L0();
        if (more) { tile_issue(blk + 1, 0, 2 * kb); WAIT_V(12); }
        else if (kb == 0) WAIT_V(8);
        else WAIT_V(0);
      }
#pragma unroll
      for (int t = 0; t < 8; ++t) vhi[t] = lds_tr(smem, av[t] + tb + slot_off(2 * kb + 1));
      WAIT_L0();
      if (br && more) tile_issue(blk + 1, 0, 2 * kb + 1);
#pragma unroll
      for (int t = 0; t < 8; ++t)
#pragma unroll
        for (int v = 0; v < 2; ++v) o[t][v] = mfma16(cat8(vlo[t], vhi[t]), pf[kb][v], o[t][v]);
      SB();
    }
    STAMPA(2 + blk);
  }
  if (!br) BAR();
  ls[0] = xg_sum(ls[0]);
  ls[1] = xg_sum(ls[1]);
  if (dh) {
#pragma unroll
    for (int t = 0; t < 8; ++t) { const f32x4 tmp = o[t][0]; o[t][0] = o[t][1]; o[t][1] = tmp; }
    float tm = m[0]; m[0] = m[1]; m[1] = tm;
    tm = ls[0]; ls[0] = ls[1]; ls[1] = tm;
  }
  {
#pragma unroll
    for (int t = 0; t < 8; ++t) *(f32x4*)(smem + ring + t * 1024 + l * 16) = o[t][1];
    f32x2 ml = {m[1], ls[1]};
    *(f32x2*)(smem + ring + 8192 + l * 8) = ml;
  }
  WAIT_L0();
  BAR();
  {
    const int pw = w ^ 4;
    const unsigned pring = (unsigned)((pw >= 4 ? pw - 4 : pw < 2 ? pw + 4 : pw + 6) * 16384);
    const f32x2 ml = *(const f32x2*)(smem + pring + 8192 + l * 8);
    const float mo = m[0], lo = ls[0];
    const float mm = fmaxf(mo, ml[0]);
    float a0 = __builtin_amdgcn_exp2f(mo - mm), a1 = __builtin_amdgcn_exp2f(ml[0] - mm);
    const float inv = 1.0f / (lo * a0 + ml[1] * a1);
    a0 *= inv; a1 *= inv;
    const __amdgpu_buffer_rsrc_t ro = __builtin_amdgcn_make_buffer_rsrc(ows + (size_t)br * (4u << 20) + ((size_t)(2 * b + dh) * 128 + head * 4) * 1024, 0, 4096, 0x00020000);
#pragma unroll
    for (int t = 0; t < 8; ++t) {
      const f32x4 oth = *(const f32x4*)(smem + pring + t * 1024 + l * 16);
      const f32x4 r = o[t][0] * a0 + oth * a1;
      u32x2 pkd = {pk2(r[0], r[1]), pk2(r[2], r[3])};
      __builtin_amdgcn_raw_buffer_store_b64(pkd, ro, t * 512 + ((g >> 1) * 16 + c) * 16 + (g & 1) * 8, 0, 16);
    }
  }
  WAIT_V(0);
  WAIT_L0();
  BAR();
  if (tid == 0) __hip_atomic_fetch_add(cnt + ((br * 4 + (b >> 2)) * 4 + (head >> 3)) * 32, 1u, RLX_AGENT);
  STAMPA(6);

  {
    const int kq = xcd >> 1, mt = idx & 3, nl = idx >> 2, nt = (xcd & 1) * 4 + nl;
    if (WOPF && !br && w < 4) {
      unsigned vp[8];
#pragma unroll
      for (int i = 0; i < 8; ++i) vp[i] = l * 16 + i * 1024;
      const char* src = woB + (size_t)(kq * 1024 + idx * 64 + w * 16) * 2048 + (xcd & 1) * 1024 + 4096;
      glds8<2>(src, vp, lbase + 98304 + w * 16384 + 4096);
      glds8<2>(src + 8 * 2048, vp, lbase + 98304 + w * 16384 + 8192 + 4096);
      WAIT_V(0);
    }
    const char* og = ows + (size_t)br * (4u << 20) + ((size_t)(mt * 8) * 128 + kq * 32) * 1024;
    const char* wo = (br ? woB : woA) + (size_t)(kq * 1024) * 2048 + nt * 256;
    f32x4 acc[4][4];
    gemm_core<2048, 1, false>(smem, lbase, wo, og, 4 * ((mt + nl) & 3), w, l, acc, [](int) {}, false, cnt + ((br * 4 + mt) * 4 + kq) * 32 GSPASS2);
    STAMPA(7);
    const int tile = br * 32 + nt * 4 + mt;
    const __amdgpu_buffer_rsrc_t rs = __builtin_amdgcn_make_buffer_rsrc(ows + PART_OFF + (size_t)tile * (16 * 16384), 0, 16 * 16384, 0x00020000);
    unsigned* rcnt = cnt + (32 + tile * 4) * 32;
    if (w < 4) {
      if (w != kq) {
        const int off = (w * 4 + kq) * 16384 + l * 16;
#pragma unroll
        for (int t = 0; t < 4; ++t)
#pragma unroll
          for (int v = 0; v < 4; ++v) __builtin_amdgcn_raw_buffer_store_b128(__builtin_bit_cast(u32x4, acc[t][v]), rs, off + (t * 4 + v) * 1024, 0, 16);
        WAIT_V(0);
        if (l == 0) __hip_atomic_fetch_add(rcnt + w * 32, 1u, RLX_AGENT);
      } else {
#pragma unroll
        for (int t = 0; t < 4; ++t)
#pragma unroll
          for (int v = 0; v < 4; ++v) *(f32x4*)(smem + (t * 4 + v) * 1024 + l * 16) = acc[t][v];
        WAIT_L0();
      }
    } else if (w == 4) {
      poll_ge(rcnt + kq * 32, 3u);
    }
    asm volatile("" ::: "memory");
    BAR();
    asm volatile("" ::: "memory");
    u32x4 pr[3][2];
#pragma unroll
    for (int s = 0; s < 3; ++s) {
      const int src = s + (s >= kq);
#pragma unroll
      for (int i = 0; i < 2; ++i) pr[s][i] = __builtin_amdgcn_raw_buffer_load_b128(rs, (kq * 4 + src) * 16384 + (2 * w + i) * 1024 + l * 16, 0, 16);
    }
#pragma unroll
    for (int i = 0; i < 2; ++i) {
      const f32x4 own = *(const f32x4*)(smem + (2 * w + i) * 1024 + l * 16);
      const f32x4 r = (__builtin_bit_cast(f32x4, pr[0][i]) + __builtin_bit_cast(f32x4, pr[1][i])) + (__builtin_bit_cast(f32x4, pr[2][i]) + own);
      const int mm = mt * 128 + (kq >> 1) * 64 + ((w & 1) * 2 + i) * 16 + c, nn = nt * 128 + (kq & 1) * 64 + (w >> 1) * 16 + g * 4;
      u32x2 pkd = {pk2(r[0], r[1]), pk2(r[2], r[3])};
      *(u32x2*)(out + ((size_t)mm * 2048 + br * 1024 + nn) * 2) = pkd;
    }
  }
  STAMPA(8);
  STAMP_FLUSH(ows);
}

extern "C" void kernel_launch(void* const* d_in, const int* in_sizes, int n_in, void* d_out, int out_size, void* d_ws, size_t ws_size,
                              hipStream_t stream) {
  k1_qproj_attn<<<256, 512, 0, stream>>>((const char*)d_in[0], (const char*)d_in[1], (const char*)d_in[2], (const char*)d_in[3], (const char*)d_in[5],
                                         (const char*)d_in[6], (const char*)d_in[7], (const char*)d_in[8], (char*)d_ws, (const char*)d_in[4],
                                         (const char*)d_in[9], (char*)d_out);
}
```

```cpp
#include <hip/hip_runtime.h>
#include <hip/hip_bf16.h>

#define POL_Q 2
#define POL_KVA 2

typedef short bf16x8 __attribute__((ext_vector_type(8)));
typedef short s16x4 __attribute__((ext_vector_type(4)));
typedef float f32x4 __attribute__((ext_vector_type(4)));
typedef float f32x2 __attribute__((ext_vector_type(2)));
typedef __bf16 bf16x2 __attribute__((ext_vector_type(2)));
typedef unsigned u32x4 __attribute__((ext_vector_type(4)));
typedef unsigned u32x2 __attribute__((ext_vector_type(2)));
#define LAS __attribute__((address_space(3)))
#define DI __device__ __forceinline__

#define WAIT_V(n) asm volatile("s_waitcnt vmcnt(%0)" ::"n"(n) : "memory")
#define WAIT_L0() do { asm volatile("" ::: "memory"); __builtin_amdgcn_s_waitcnt(0xC07F); asm volatile("" ::: "memory"); } while (0)
#define BAR() __builtin_amdgcn_s_barrier()
#define SB() __builtin_amdgcn_sched_barrier(0)

template <bool NT>
DI void glds(const char* sbase, unsigned voff, unsigned ldsaddr) {
  if (NT)
    asm volatile("s_mov_b32 m0, %2\n\ts_nop 0\n\tglobal_load_lds_dwordx4 %0, %1 nt" ::"v"(voff), "s"(sbase), "s"(ldsaddr) : "memory");
  else
    asm volatile("s_mov_b32 m0, %2\n\ts_nop 0\n\tglobal_load_lds_dwordx4 %0, %1" ::"v"(voff), "s"(sbase), "s"(ldsaddr) : "memory");
}
template <bool NT>
DI void glds4(const char* sbase, unsigned v0, unsigned v1, unsigned v2, unsigned v3, unsigned ldsaddr) {
  if (NT)
    asm volatile("s_mov_b32 m0, %5\n\ts_nop 0\n\t"
                 "global_load_lds_dwordx4 %0, %4 nt\n\t"
                 "global_load_lds_dwordx4 %1, %4 offset:1024 nt\n\t"
                 "global_load_lds_dwordx4 %2, %4 offset:2048 nt\n\t"
                 "global_load_lds_dwordx4 %3, %4 offset:3072 nt"
                 ::"v"(v0), "v"(v1), "v"(v2), "v"(v3), "s"(sbase), "s"(ldsaddr) : "memory");
  else
    asm volatile("s_mov_b32 m0, %5\n\ts_nop 0\n\t"
                 "global_load_lds_dwordx4 %0, %4\n\t"
                 "global_load_lds_dwordx4 %1, %4 offset:1024\n\t"
                 "global_load_lds_dwordx4 %2, %4 offset:2048\n\t"
                 "global_load_lds_dwordx4 %3, %4 offset:3072"
                 ::"v"(v0), "v"(v1), "v"(v2), "v"(v3), "s"(sbase), "s"(ldsaddr) : "memory");
}
DI void pin(const char* sbase, unsigned voff, unsigned ldsaddr) {
  asm volatile("s_mov_b32 m0, %2\n\ts_nop 0\n\tglobal_load_lds_dword %0, %1" ::"v"(voff), "s"(sbase), "s"(ldsaddr) : "memory");
}

DI f32x4 mfma16(bf16x8 a, bf16x8 b, f32x4 c) { return __builtin_amdgcn_mfma_f32_16x16x32_bf16(a, b, c, 0, 0, 0); }
DI unsigned pk2(float lo, float hi) { f32x2 a = {lo, hi}; return __builtin_bit_cast(unsigned, __builtin_convertvector(a, bf16x2)); }
DI bf16x8 lds_b128(const char* smem, unsigned off) { return *(const bf16x8*)(smem + off); }
DI s16x4 lds_tr(char* smem, unsigned off) { return __builtin_amdgcn_ds_read_tr16_b64_v4i16((LAS s16x4*)(smem + off)); }
DI bf16x8 cat8(s16x4 lo, s16x4 hi) { return __builtin_shufflevector(lo, hi, 0, 1, 2, 3, 4, 5, 6, 7); }
DI float xg_max(float x) {
  auto s = __builtin_amdgcn_permlane16_swap(__float_as_uint(x), __float_as_uint(x), false, false);
  x = fmaxf(__uint_as_float(s[0]), __uint_as_float(s[1]));
  auto t = __builtin_amdgcn_permlane32_swap(__float_as_uint(x), __float_as_uint(x), false, false);
  return fmaxf(__uint_as_float(t[0]), __uint_as_float(t[1]));
}
DI float xg_sum(float x) {
  auto s = __builtin_amdgcn_permlane16_swap(__float_as_uint(x), __float_as_uint(x), false, false);
  x = __uint_as_float(s[0]) + __uint_as_float(s[1]);
  auto t = __builtin_amdgcn_permlane32_swap(__float_as_uint(x), __float_as_uint(x), false, false);
  return __uint_as_float(t[0]) + __uint_as_float(t[1]);
}

#define STAMP(k)
#define STAMP_DECL
#define STAMP_FLUSH(ws)
constexpr float SL2E = 0.088388347648318447f * 1.4426950408889634f;

constexpr size_t PART_OFF = 16u << 20, CNT_OFF = 8u << 20, Q_OFF = 10u << 20;
#define ASLEEP 0

#define G8_(j, off, sfx) "global_load_lds_dwordx4 %" #j ", %8 offset:" #off sfx "\n\t"
template <int MODE>
DI void glds8(const char* sbase, const unsigned (&v)[8], unsigned ldsaddr) {
  if (MODE == 0)
    asm volatile("s_mov_b32 m0, %9\n\ts_nop 0\n\t" G8_(0, -4096, "") G8_(1, -3072, "") G8_(2, -2048, "") G8_(3, -1024, "") G8_(4, 0, "") G8_(5, 1024, "") G8_(6, 2048, "") G8_(7, 3072, "")
                 ::"v"(v[0]), "v"(v[1]), "v"(v[2]), "v"(v[3]), "v"(v[4]), "v"(v[5]), "v"(v[6]), "v"(v[7]), "s"(sbase), "s"(ldsaddr) : "memory");
  else if (MODE == 3)
    asm volatile("s_mov_b32 m0, %9\n\ts_nop 0\n\t" G8_(0, -4096, " sc1") G8_(1, -3072, " sc1") G8_(2, -2048, " sc1") G8_(3, -1024, " sc1") G8_(4, 0, " sc1") G8_(5, 1024, " sc1") G8_(6, 2048, " sc1") G8_(7, 3072, " sc1")
                 ::"v"(v[0]), "v"(v[1]), "v"(v[2]), "v"(v[3]), "v"(v[4]), "v"(v[5]), "v"(v[6]), "v"(v[7]), "s"(sbase), "s"(ldsaddr) : "memory");
  else if (MODE == 1)
    asm volatile("s_mov_b32 m0, %9\n\ts_nop 0\n\t" G8_(0, -4096, " nt") G8_(1, -3072, " nt") G8_(2, -2048, " nt") G8_(3, -1024, " nt") G8_(4, 0, " nt") G8_(5, 1024, " nt") G8_(6, 2048, " nt") G8_(7, 3072, " nt")
                 ::"v"(v[0]), "v"(v[1]), "v"(v[2]), "v"(v[3]), "v"(v[4]), "v"(v[5]), "v"(v[6]), "v"(v[7]), "s"(sbase), "s"(ldsaddr) : "memory");
  else
    asm volatile("s_mov_b32 m0, %9\n\ts_nop 0\n\t" G8_(0, -4096, " nt") G8_(0, -4096, "") G8_(1, -3072, " nt") G8_(1, -3072, "") G8_(2, -2048, " nt") G8_(2, -2048, "") G8_(3, -1024, " nt") G8_(3, -1024, "")
                 G8_(4, 0, " nt") G8_(4, 0, "") G8_(5, 1024, " nt") G8_(5, 1024, "") G8_(6, 2048, " nt") G8_(6, 2048, "") G8_(7, 3072, " nt") G8_(7, 3072, "")
                 ::"v"(v[0]), "v"(v[1]), "v"(v[2]), "v"(v[3]), "v"(v[4]), "v"(v[5]), "v"(v[6]), "v"(v[7]), "s"(sbase), "s"(ldsaddr) : "memory");
}
#define CVAR 1
#define RPG 2
#define H0SLEEP 12
#define KVAPOL 0
#define BLKBAR 0
#define BLKBAR2 0
#define BNT 1
#define FNT 1
#define WOPF 1
#define NT_VB 1
#define NT_KB 1
#define NT_A 1
#define TLBW 0
#define OAGATE 0
#define PRIO 0
#define FDUP 1
#define GSARG
#define GSPASS
#define GSPASS2
#define GBAR() BAR()
#define RLX_AGENT __ATOMIC_RELAXED, __HIP_MEMORY_SCOPE_AGENT
DI void poll_ge(unsigned* p, unsigned want) {
  unsigned spins = 0;
  while (__hip_atomic_load(p, RLX_AGENT) < want && ++spins < (1u << 22)) __builtin_amdgcn_s_sleep(1);
  asm volatile("" ::: "memory");
}
DI void tile_ld(u32x4 (&r)[4], const char* sbase, unsigned v0, unsigned v1, unsigned v2, unsigned v3) {
  asm volatile("global_load_dwordx4 %0, %4, %8 nt\n\tglobal_load_dwordx4 %1, %5, %8 offset:1024 nt\n\t"
               "global_load_dwordx4 %2, %6, %8 offset:2048 nt\n\tglobal_load_dwordx4 %3, %7, %8 offset:3072 nt"
               : "=&v"(r[0]), "=&v"(r[1]), "=&v"(r[2]), "=&v"(r[3]) : "v"(v0), "v"(v1), "v"(v2), "v"(v3), "s"(sbase) : "memory");
}
struct HFrags { s16x4 a[4][2]; bf16x8 b[4]; };
template <int WS, int XT, bool TAIL, class TailF, class LandF, class DumpF>
DI void gemm_core(char* smem, unsigned lbase, const char* wg, const char* xg, int rot, int w, int l, f32x4 (&acc)[4][4], TailF tail, LandF land, DumpF dump, bool dotail, unsigned* gate GSARG) {
  int bn_ = 1; (void)bn_;
  const int c = l & 15, g = l >> 4, q4 = c >> 2, p = l & 3;
  if (w >= 4) {
    u32x4 lr[10][4];
    if (TAIL && !dotail) {
#pragma unroll
      for (int k = 0; k < 10; ++k) land(k, lr[k]);
      WAIT_V(0);
    }
    const int iw = w & 1;
    unsigned vw[8], vx[8];
    {
      const unsigned vw0 = (unsigned)(g * WS + ((c ^ (2 * g)) * 16)), vw1 = vw0 ^ 128;
      const unsigned vx0 = XT ? (unsigned)(l * 16) : (unsigned)((l >> 3) * 2048 + (((l & 7) ^ g) * 16));
      const unsigned vx1 = XT ? vx0 : (vx0 ^ 64);
#pragma unroll
      for (int j = 0; j < 8; ++j) {
        vw[j] = ((j & 2) ? vw1 : vw0) + j * (4 * WS - 1024);
        vx[j] = XT ? vx0 + (j >> 1) * (128 * 1024 - 2048) : ((j & 1) ? vx1 : vx0) + j * (16384 - 1024);
      }
    }
    const char* w_w = wg + (size_t)(32 * iw) * WS + 4096;
    const char* x_w = (XT ? xg + (size_t)(4 * iw) * (128 * 1024) : xg + (size_t)(64 * iw) * 2048) + 4096;
    const unsigned lw0 = lbase + iw * 8192 + 4096;
    auto wpieces = [&](int kap, unsigned bufoff, bool first) {
      if (first && FNT) glds8<FDUP ? 2 : 1>(w_w + (size_t)kap * (64 * WS), vw, lw0 + bufoff);
      else glds8<0>(w_w + (size_t)kap * (64 * WS), vw, lw0 + bufoff);
    };
    auto xpieces = [&](int kap, unsigned bufoff) { glds8<XT ? 3 : 0>(x_w + kap * (XT ? 2048 : 128), vx, lw0 + bufoff + 16384); };
    constexpr int FN = (FDUP && FNT) ? 24 : 16;
    if (w < 6) {
      if (XT) {
#pragma unroll 1
        for (int f = 0; f < 2; ++f) wpieces(rot + f, f * 32768, true);
        if (w == 4) poll_ge(gate, 8u);
        BAR();
#pragma unroll 1
        for (int f = 0; f < 2; ++f) xpieces(rot + f, f * 32768);
        WAIT_V(8);
      } else {
#pragma unroll 1
        for (int f = 0; f < 2; ++f) { wpieces(rot + f, f * 32768, true); xpieces(rot + f, f * 32768); }
      }
#pragma unroll 1
      for (int i = 0; i < 2; ++i) {
        WAIT_V(FN);
        GBAR();
        GBAR();
        wpieces(rot + i + 2, i * 32768, true); xpieces(rot + i + 2, i * 32768);
        GBAR();
        GBAR();
      }
      WAIT_V(FN);
      GBAR();
      GBAR();
      if (TAIL && dotail) tail(0);
      GBAR();
      if (TAIL && dotail) tail(1);
      GBAR();
      if (TAIL && dotail) { tail(2); WAIT_V(12); } else WAIT_V(0);
      GBAR();
      if (TAIL && dotail) tail(3);
      GBAR();
      GBAR();
      GBAR();
    } else {
      auto stage = [&](int kap, int hb) { wpieces(kap & 15, 65536 + hb * 32768, false); xpieces(kap & 15, 65536 + hb * 32768); };
      __builtin_amdgcn_s_sleep(H0SLEEP);
      if (XT) { wpieces((rot + 4) & 15, 65536, false); BAR(); xpieces((rot + 4) & 15, 65536); }
      else stage(rot + 4, 0);
      GBAR();
      stage(rot + 8, 1);
      WAIT_V(16);
      GBAR();
      stage(rot + 12, 2);
#pragma unroll 1
      for (int i = 0; i < 3; ++i) {
        WAIT_V(16);
        GBAR();
        stage(rot + 5 + i, 0);
        WAIT_V(16);
        GBAR();
        stage(rot + 9 + i, 1);
        GBAR();
        stage(rot + 13 + i, 2);
        WAIT_V(32);
        GBAR();
      }
      if (TAIL && dotail) { tail(0); tail(1); WAIT_V(24); } else WAIT_V(16);
      GBAR();
      if (TAIL && dotail) { tail(2); tail(3); WAIT_V(16); } else WAIT_V(0);
      GBAR();
    }
    if (TAIL && !dotail) {
#pragma unroll
      for (int k = 0; k < 8; ++k) dump(k, lr[k]);
      BAR();
      dump(8, lr[8]); dump(9, lr[9]);
      asm volatile("s_waitcnt lgkmcnt(0)" ::: "memory");
    }
  } else {
    if (XT) BAR();
    const int nh = w & 1, mh = w >> 1;
    unsigned aw[4];
    {
      const int F = 2 * q4 | 8 * (g & 1);
#pragma unroll
      for (int t = 0; t < 4; ++t) aw[t] = (unsigned)((8 * g + q4) * 256 + ((((2 * (4 * nh + t)) ^ F) + (p >> 1)) * 16) + (p & 1) * 8);
    }
    const unsigned ax0 = XT ? (unsigned)(16384 + mh * 8192 + l * 16) : (unsigned)(16384 + (64 * mh + c) * 128 + ((g ^ (c >> 1)) * 16));
    const unsigned ax1 = XT ? ax0 + 1024 : (ax0 ^ 64);
    struct FF { HFrags h[2]; };
    auto mm = [&](const FF& fo) {
#pragma unroll
      for (int ks = 0; ks < 2; ++ks)
#pragma unroll
        for (int t = 0; t < 4; ++t)
#pragma unroll
          for (int v = 0; v < 4; ++v) acc[t][v] = mfma16(cat8(fo.h[ks].a[t][0], fo.h[ks].a[t][1]), fo.h[ks].b[v], acc[t][v]);
    };
    auto full = [&](FF& fn, const FF& fo, unsigned bo) {
#pragma unroll
      for (int ks = 0; ks < 2; ++ks)
#pragma unroll
        for (int t = 0; t < 4; ++t) {
          fn.h[ks].b[t] = lds_b128(smem, (ks ? ax1 : ax0) + bo + t * 2048);
          fn.h[ks].a[t][0] = lds_tr(smem, aw[t] + bo + ks * 8192);
          fn.h[ks].a[t][1] = lds_tr(smem, aw[t] + bo + ks * 8192 + 1024);
        }
      mm(fo);
#pragma unroll
      for (int t = 0; t < 24; ++t) {
        __builtin_amdgcn_sched_group_barrier(0x008, 1, 0);
        __builtin_amdgcn_sched_group_barrier(0x100, 1, 0);
      }
      __builtin_amdgcn_sched_group_barrier(0x008, 8, 0);
      SB();
    };
#pragma unroll
    for (int t = 0; t < 4; ++t)
#pragma unroll
      for (int v = 0; v < 4; ++v) acc[t][v] = f32x4{0, 0, 0, 0};
    FF f0, f1;
#pragma unroll
    for (int ks = 0; ks < 2; ++ks)
#pragma unroll
      for (int t = 0; t < 4; ++t) { f1.h[ks].b[t] = bf16x8{0, 0, 0, 0, 0, 0, 0, 0}; f1.h[ks].a[t][0] = s16x4{0, 0, 0, 0}; f1.h[ks].a[t][1] = s16x4{0, 0, 0, 0}; }
#pragma unroll 1
    for (int n = 0; n < 16; n += 2) {
      const unsigned bo0 = (n & 2) ? 98304 : ((n >> 2) & 1) * 32768, bo1 = (n & 2) ? 131072 : 65536;
      WAIT_L0();
      GBAR();
      full(f0, f1, bo0);
      WAIT_L0();
      GBAR();
      full(f1, f0, bo1);
    }
    WAIT_L0();
    if (TAIL && !dotail) BAR();
    mm(f1);
  }
}

__global__ __launch_bounds__(512, 2) void k1_qproj_attn(const char* __restrict__ xA, const char* __restrict__ kA, const char* __restrict__ vA,
                                                        const char* __restrict__ wqA, const char* __restrict__ xB, const char* __restrict__ kB,
                                                        const char* __restrict__ vB, const char* __restrict__ wqB, char* __restrict__ ows,
                                                        const char* __restrict__ woA, const char* __restrict__ woB, char* __restrict__ out) {
  __shared__ __attribute__((aligned(1024))) char smem[163840];
  const unsigned lbase = (unsigned)(size_t)(LAS char*)smem;
  const int tid = threadIdx.x, l = tid & 63, w = __builtin_amdgcn_readfirstlane(tid >> 6);
  const int xcd = blockIdx.x & 7, j = blockIdx.x >> 3;
  const int br = j & 1, idx = j >> 1;
  const int bg = idx & 3, qhead = 4 * xcd + (idx >> 2), rot = 4 * (((idx >> 2) + bg) & 3);
  const char* xg = (br ? xB : xA) + (size_t)bg * 128 * 2048;
  const char* wq = (br ? wqB : wqA) + qhead * 256;
  const int c = l & 15, g = l >> 4, q4 = c >> 2, p = l & 3;
  const int u = w & 3, dh = w >> 2;
  const int b = br ? 4 * bg + u : (xcd & 1) * 8 + (idx >> 1);
  const int head = br ? qhead : 8 * (xcd >> 1) + 4 * (idx & 1) + u;
  const unsigned ring = (unsigned)((w >= 4 ? w - 4 : w < 2 ? w + 4 : w + 6) * 16384);
  STAMP_DECL;
  STAMP(0);
  if (ASLEEP && !br) {
#pragma unroll 1
    for (int i = 0; i < ASLEEP; ++i) __builtin_amdgcn_s_sleep(16);
  }
  const size_t kvoff = ((size_t)(br ? b * 32 + head : b * 4 + (xcd >> 1)) * 512 + dh * 256) * 256;
  const char* kg = (br ? kB : kA) + kvoff;
  const char* vg = (br ? vB : vA) + kvoff;
  unsigned vk[4], vv[2];
#pragma unroll
  for (int i = 0; i < 4; ++i) vk[i] = (unsigned)(g * 256 + ((c ^ g ^ (4 * i)) * 16));
#pragma unroll
  for (int i = 0; i < 2; ++i) vv[i] = (unsigned)(g * 256 + ((c ^ (2 * g) ^ (8 * i)) * 16));
  auto slot_off = [](int t) constexpr { return t * 4096; };
  auto tile_to = [&](int blk, int isV, int t, unsigned base) {
    const unsigned la = lbase + base + t * 4096;
    const char* src = (isV ? vg : kg) + (blk * 4 + t) * 4096;
    const bool nt = br ? (isV ? NT_VB : NT_KB) : NT_A;
    if (nt) {
      if (isV) glds4<true>(src, vv[0], vv[1], vv[0], vv[1], la);
      else glds4<true>(src, vk[0], vk[1], vk[2], vk[3], la);
    } else {
      if (isV) glds4<false>(src, vv[0], vv[1], vv[0], vv[1], la);
      else glds4<false>(src, vk[0], vk[1], vk[2], vk[3], la);
    }
  };
  auto tile_issue = [&](int blk, int isV, int t) { tile_to(blk, isV, t, ring); };

  unsigned* ctl = (unsigned*)(ows + CNT_OFF);
  const __amdgpu_buffer_rsrc_t rc = __builtin_amdgcn_make_buffer_rsrc(ows + CNT_OFF, 0, 65536 + 16384, 0x00020000);
  unsigned nonce = 0;
  auto echo = [&]() {
    const u32x4 r = __builtin_amdgcn_raw_buffer_load_b128(rc, l * 16, 0, 16);
    __builtin_amdgcn_raw_buffer_store_b128(r, rc, 4096 + l * 16, 0, 16);
  };
  if (w == 0) {
    nonce = __hip_atomic_load(ctl + 1024 + blockIdx.x, RLX_AGENT) + 1u;
    if (l == 0) __hip_atomic_store(ctl + blockIdx.x, nonce, RLX_AGENT);
  } else if (w == 1 && blockIdx.x == 0) {
#pragma unroll
    for (int i = 0; i < 6; ++i) __builtin_amdgcn_raw_buffer_store_b32(0u, rc, 8192 + (i * 64 + l) * 128, 0, 16);
    WAIT_V(0);
    __builtin_amdgcn_s_sleep(24);
    echo();
  }
  if (TLBW && w == 2) {
    const int kq_ = xcd >> 1;
    const char* a;
    const int uu = l & 3;
    const size_t kvo = ((size_t)(br ? (4 * bg + uu) * 32 + qhead : b * 4 + kq_) * 512) * 256;
    if (l < 4) a = (br ? kB : kA) + kvo;
    else if (l < 8) a = (br ? vB : vA) + kvo;
    else if (l < 10) a = (br ? woB : woA) + (size_t)(kq_ * 1024 + (l & 1) * 1023) * 2048;
    else if (l < 12) a = ows + (size_t)br * (4u << 20) + (l & 1) * (2u << 20) + (12u << 10);
    else if (l < 14) a = ows + Q_OFF + (l & 1) * (2u << 20);
    else if (l < 15) a = ows + PART_OFF + (size_t)(br * 32 + ((xcd & 1) * 4 + (idx >> 2)) * 4 + (idx & 3)) * (16 * 16384);
    else a = out;
    if (l < 16) { unsigned t_ = __builtin_nontemporal_load((const unsigned*)a); asm volatile("" ::"v"(t_)); }
  }
  unsigned* cnt = ctl + 2048 + 32;
  auto confirm = [&]() {
    unsigned spins = 0;
    while (__hip_atomic_load(ctl + 1024 + blockIdx.x, RLX_AGENT) != nonce && ++spins < (1u << 18)) __builtin_amdgcn_s_sleep(2);
    if (l == 0) __hip_atomic_fetch_add(ctl + 2048, 1u, RLX_AGENT);
  };
  f32x4 qa[4][4];
  auto land = [&](int k, u32x4 (&r)[4]) {
    const int ph = k >> 1, hf = k & 1;
    const char* src = ((ph & 1) ? vg : kg) + (hf - 1) * 65536 + ((ph >> 1) * 4 + u) * 4096;
    if (ph & 1) tile_ld(r, src, vv[0], vv[1], vv[0], vv[1]);
    else tile_ld(r, src, vk[0], vk[1], vk[2], vk[3]);
  };
  auto dump = [&](int k, const u32x4 (&r)[4]) {
    const unsigned d = k * 16384 + u * 4096 + l * 16;
    asm volatile("ds_write_b128 %0, %1\n\tds_write_b128 %0, %2 offset:1024\n\tds_write_b128 %0, %3 offset:2048\n\tds_write_b128 %0, %4 offset:3072"
                 ::"v"(lbase + d), "v"(r[0]), "v"(r[1]), "v"(r[2]), "v"(r[3]) : "memory");
  };
  gemm_core<8192, 0, true>(smem, lbase, wq, xg, rot, w, l, qa, [&](int n) { tile_issue(0, 0, n); }, land, dump, br != 0, nullptr GSPASS);
  bf16x8 qf[2][4];
  if (br) {
    if (w < 4) {
#pragma unroll
      for (int t = 0; t < 4; ++t)
#pragma unroll
        for (int v = 0; v < 4; ++v) {
          const int tau = 4 * (w & 1) + t;
          u32x2 pkd = {pk2(qa[t][v][0], qa[t][v][1]), pk2(qa[t][v][2], qa[t][v][3])};
          const unsigned off = 98304 + ((w >> 1) * 4 + v) * 4096 + (tau >> 1) * 1024 + ((2 * (tau & 1) + (g >> 1)) * 16 + c) * 16 + (g & 1) * 8;
          *(u32x2*)(smem + off) = pkd;
        }
    }
    WAIT_L0();
    BAR();
#pragma unroll
    for (int v = 0; v < 2; ++v)
#pragma unroll
      for (int s = 0; s < 4; ++s) qf[v][s] = lds_b128(smem, 98304 + u * 8192 + v * 4096 + s * 1024 + l * 16);
    if (w == 0) confirm();
    if (w < 4) {
#pragma unroll 1
      for (int t = 0; t < 4; ++t) tile_issue(0, 0, t);
    }
  } else {
    const __amdgpu_buffer_rsrc_t rq = __builtin_amdgcn_make_buffer_rsrc(ows + Q_OFF, 0, 4u << 20, 0x00020000);
    if (w < 4) {
      const int qo = ((bg * 8 + (w >> 1) * 4) * 128 + qhead * 4 + (w & 1) * 2) * 1024 + ((g >> 1) * 16 + c) * 16 + (g & 1) * 8;
#pragma unroll
      for (int t = 0; t < 4; ++t)
#pragma unroll
        for (int v = 0; v < 4; ++v) {
          u32x2 pkd = {pk2(qa[t][v][0], qa[t][v][1]), pk2(qa[t][v][2], qa[t][v][3])};
          __builtin_amdgcn_raw_buffer_store_b64(pkd, rq, qo + v * (128 * 1024) + t * 512, 0, 16);
        }
      if (w == 1 && blockIdx.x == 0) echo();
      if (w == 0) confirm();
      WAIT_V(0);
    }
    BAR();
    if (tid == 0) __hip_atomic_fetch_add(cnt + (288 + xcd * 4 + bg) * 32, 1u, RLX_AGENT);
    if (w == 1 && blockIdx.x == 0) {
      unsigned spins = 0;
      while (__hip_atomic_load(ctl + 2048, RLX_AGENT) < 256u && ++spins < (1u << 17)) { echo(); __builtin_amdgcn_s_sleep(8); }
    }
    if (w == 4) poll_ge(cnt + (288 + (head >> 2) * 4 + (b >> 2)) * 32, 4u);
    BAR();
#pragma unroll
    for (int v = 0; v < 2; ++v)
#pragma unroll
      for (int s = 0; s < 4; ++s)
        qf[v][s] = __builtin_bit_cast(bf16x8, __builtin_amdgcn_raw_buffer_load_b128(rq, ((2 * b + v) * 128 + head * 4 + s) * 1024 + l * 16, 0, 16));
    WAIT_V(0);
  }
  STAMP(1);
#define STAMPA(k) STAMP(k)

  unsigned ak[4], av[8];
#pragma unroll
  for (int s = 0; s < 4; ++s) ak[s] = (unsigned)(c * 256 + ((g ^ c ^ (4 * s)) * 16));
  {
    const int row = 4 * g + q4, F = 2 * (row & 7);
#pragma unroll
    for (int t = 0; t < 8; ++t) av[t] = (unsigned)(row * 256 + ((((2 * t) ^ F) + (p >> 1)) * 16) + (p & 1) * 8);
  }
  f32x4 o[8][2];
#pragma unroll
  for (int t = 0; t < 8; ++t) { o[t][0] = f32x4{0, 0, 0, 0}; o[t][1] = f32x4{0, 0, 0, 0}; }
  unsigned tb = ring, pnext = dh * 16384u, pfree = 4 * 32768u + dh * 16384u;
  float m[2] = {-__builtin_inff(), -__builtin_inff()}, ls[2] = {0.f, 0.f};
#pragma unroll 1
  for (int blk = 0; blk < 4; ++blk) {
    f32x4 sc[4][2];
    if (PRIO && br) {
      if (PRIO == 1) { if (blk == 0) __builtin_amdgcn_s_setprio(3); else if (blk == 1) __builtin_amdgcn_s_setprio(2); else if (blk == 2) __builtin_amdgcn_s_setprio(1); else __builtin_amdgcn_s_setprio(0); }
      else { if (blk == 0) __builtin_amdgcn_s_setprio(3); else if (blk == 2) __builtin_amdgcn_s_setprio(2); else if (blk == 3) __builtin_amdgcn_s_setprio(0); }
    }
    if (!br) {
      if (blk < 3) WAIT_V(12); else WAIT_V(4);
      BAR();
      if (blk == 1) tile_to(blk + 2, 0, u, pfree);
      pfree = tb; tb = pnext; pnext = pnext >= 4 * 32768u ? pnext - 4 * 32768u : pnext + 32768u;
    }
#pragma unroll
    for (int t = 0; t < 4; ++t) {
      if (br) WAIT_V(12);
      bf16x8 kf[4];
#pragma unroll
      for (int s = 0; s < 4; ++s) kf[s] = lds_b128(smem, ak[s] + tb + slot_off(t));
      WAIT_L0();
      if (br) tile_issue(blk, 1, t);
      sc[t][0] = f32x4{0, 0, 0, 0}; sc[t][1] = f32x4{0, 0, 0, 0};
#pragma unroll
      for (int s = 0; s < 4; ++s)
#pragma unroll
        for (int v = 0; v < 2; ++v) sc[t][v] = mfma16(kf[s], qf[v][s], sc[t][v]);
      SB();
    }
    bf16x8 pf[2][2];
#pragma unroll
    for (int v = 0; v < 2; ++v) {
      float mx = sc[0][v][0];
#pragma unroll
      for (int t = 0; t < 4; ++t)
#pragma unroll
        for (int i = 0; i < 4; ++i) mx = fmaxf(mx, sc[t][v][i]);
      mx = xg_max(mx) * SL2E;
      const float mn = fmaxf(m[v], mx);
      const float alpha = __builtin_amdgcn_exp2f(m[v] - mn);
      ls[v] *= alpha;
#pragma unroll
      for (int t = 0; t < 8; ++t) o[t][v] *= alpha;
      m[v] = mn;
      float sum = 0.f;
#pragma unroll
      for (int t = 0; t < 4; ++t)
#pragma unroll
        for (int i = 0; i < 4; ++i) { const float e = __builtin_amdgcn_exp2f(fmaf(sc[t][v][i], SL2E, -mn)); sc[t][v][i] = e; sum += e; }
      ls[v] += sum;
#pragma unroll
      for (int kb = 0; kb < 2; ++kb) {
        u32x4 pw = {pk2(sc[2 * kb][v][0], sc[2 * kb][v][1]), pk2(sc[2 * kb][v][2], sc[2 * kb][v][3]),
                    pk2(sc[2 * kb + 1][v][0], sc[2 * kb + 1][v][1]), pk2(sc[2 * kb + 1][v][2], sc[2 * kb + 1][v][3])};
        pf[kb][v] = __builtin_bit_cast(bf16x8, pw);
      }
    }
    const bool more = blk < 3;
    if (PRIO == 2 && br && blk == 2) __builtin_amdgcn_s_setprio(1);
    if (!br) {
      if (blk < 2) WAIT_V(12); else if (blk == 2) WAIT_V(8); else WAIT_V(0);
      BAR();
      pfree = tb;
      if (blk < 2) tile_to(blk + 2, 1, u, pfree);
      tb = pnext; pnext = pnext >= 4 * 32768u ? pnext - 4 * 32768u : pnext + 32768u;
      pfree = tb;
    }
#pragma unroll
    for (int kb = 0; kb < 2; ++kb) {
      s16x4 vlo[8], vhi[8];
      if (br) { if (kb == 0 || more) WAIT_V(12); else WAIT_V(4); }
#pragma unroll
      for (int t = 0; t < 8; ++t) vlo[t] = lds_tr(smem, av[t] + tb + slot_off(2 * kb));
      if (br) {
        WAIT_L0();
        if (more) { tile_issue(blk + 1, 0, 2 * kb); WAIT_V(12); }
        else if (kb == 0) WAIT_V(8);
        else WAIT_V(0);
      }
#pragma unroll
      for (int t = 0; t < 8; ++t) vhi[t] = lds_tr(smem, av[t] + tb + slot_off(2 * kb + 1));
      WAIT_L0();
      if (br && more) tile_issue(blk + 1, 0, 2 * kb + 1);
#pragma unroll
      for (int t = 0; t < 8; ++t)
#pragma unroll
        for (int v = 0; v < 2; ++v) o[t][v] = mfma16(cat8(vlo[t], vhi[t]), pf[kb][v], o[t][v]);
      SB();
    }
    STAMPA(2 + blk);
  }
  if (!br) BAR();
  ls[0] = xg_sum(ls[0]);
  ls[1] = xg_sum(ls[1]);
  if (dh) {
#pragma unroll
    for (int t = 0; t < 8; ++t) { const f32x4 tmp = o[t][0]; o[t][0] = o[t][1]; o[t][1] = tmp; }
    float tm = m[0]; m[0] = m[1]; m[1] = tm;
    tm = ls[0]; ls[0] = ls[1]; ls[1] = tm;
  }
  {
#pragma unroll
    for (int t = 0; t < 8; ++t) *(f32x4*)(smem + ring + t * 1024 + l * 16) = o[t][1];
    f32x2 ml = {m[1], ls[1]};
    *(f32x2*)(smem + ring + 8192 + l * 8) = ml;
  }
  WAIT_L0();
  BAR();
  {
    const int pw = w ^ 4;
    const unsigned pring = (unsigned)((pw >= 4 ? pw - 4 : pw < 2 ? pw + 4 : pw + 6) * 16384);
    const f32x2 ml = *(const f32x2*)(smem + pring + 8192 + l * 8);
    const float mo = m[0], lo = ls[0];
    const float mm = fmaxf(mo, ml[0]);
    float a0 = __builtin_amdgcn_exp2f(mo - mm), a1 = __builtin_amdgcn_exp2f(ml[0] - mm);
    const float inv = 1.0f / (lo * a0 + ml[1] * a1);
    a0 *= inv; a1 *= inv;
    const __amdgpu_buffer_rsrc_t ro = __builtin_amdgcn_make_buffer_rsrc(ows + (size_t)br * (4u << 20) + ((size_t)(2 * b + dh) * 128 + head * 4) * 1024, 0, 4096, 0x00020000);
#pragma unroll
    for (int t = 0; t < 8; ++t) {
      const f32x4 oth = *(const f32x4*)(smem + pring + t * 1024 + l * 16);
      const f32x4 r = o[t][0] * a0 + oth * a1;
      u32x2 pkd = {pk2(r[0], r[1]), pk2(r[2], r[3])};
      __builtin_amdgcn_raw_buffer_store_b64(pkd, ro, t * 512 + ((g >> 1) * 16 + c) * 16 + (g & 1) * 8, 0, 16);
    }
  }
  WAIT_V(0);
  WAIT_L0();
  BAR();
  if (tid == 0) __hip_atomic_fetch_add(cnt + ((br * 4 + (b >> 2)) * 4 + (head >> 3)) * 32, 1u, RLX_AGENT);
  STAMPA(6);

  {
    const int kq = xcd >> 1, mt = idx & 3, nl = idx >> 2, nt = (xcd & 1) * 4 + nl;
    if (WOPF && !br && w < 4) {
      unsigned vp[8];
#pragma unroll
      for (int i = 0; i < 8; ++i) vp[i] = l * 16 + i * 1024;
      const char* src = woB + (size_t)(kq * 1024 + idx * 64 + w * 16) * 2048 + (xcd & 1) * 1024 + 4096;
      glds8<2>(src, vp, lbase + 98304 + w * 16384 + 4096);
      glds8<2>(src + 8 * 2048, vp, lbase + 98304 + w * 16384 + 8192 + 4096);
      WAIT_V(0);
    }
    if (OAGATE && !br) {
      if (w == 4) poll_ge(cnt + ((4 + mt) * 4 + kq) * 32, OAGATE);
      BAR();
    }
    const char* og = ows + (size_t)br * (4u << 20) + ((size_t)(mt * 8) * 128 + kq * 32) * 1024;
    const char* wo = (br ? woB : woA) + (size_t)(kq * 1024) * 2048 + nt * 256;
    f32x4 acc[4][4];
    gemm_core<2048, 1, false>(smem, lbase, wo, og, 4 * ((mt + nl) & 3), w, l, acc, [](int) {}, [](int, u32x4 (&)[4]) {}, [](int, const u32x4 (&)[4]) {}, false, cnt + ((br * 4 + mt) * 4 + kq) * 32 GSPASS2);
    STAMPA(7);
    const int tile = br * 32 + nt * 4 + mt;
    const __amdgpu_buffer_rsrc_t rs = __builtin_amdgcn_make_buffer_rsrc(ows + PART_OFF + (size_t)tile * (16 * 16384), 0, 16 * 16384, 0x00020000);
    unsigned* rcnt = cnt + (32 + tile * 4) * 32;
    if (w < 4) {
      if (w != kq) {
        const int off = (w * 4 + kq) * 16384 + l * 16;
#pragma unroll
        for (int t = 0; t < 4; ++t)
#pragma unroll
          for (int v = 0; v < 4; ++v) __builtin_amdgcn_raw_buffer_store_b128(__builtin_bit_cast(u32x4, acc[t][v]), rs, off + (t * 4 + v) * 1024, 0, 16);
        WAIT_V(0);
        if (l == 0) __hip_atomic_fetch_add(rcnt + w * 32, 1u, RLX_AGENT);
      } else {
#pragma unroll
        for (int t = 0; t < 4; ++t)
#pragma unroll
          for (int v = 0; v < 4; ++v) *(f32x4*)(smem + (t * 4 + v) * 1024 + l * 16) = acc[t][v];
        WAIT_L0();
      }
    } else if (w == 4) {
      poll_ge(rcnt + kq * 32, 3u);
    }
    asm volatile("" ::: "memory");
    BAR();
    asm volatile("" ::: "memory");
    u32x4 pr[3][2];
#pragma unroll
    for (int s = 0; s < 3; ++s) {
      const int src = s + (s >= kq);
#pragma unroll
      for (int i = 0; i < 2; ++i) pr[s][i] = __builtin_amdgcn_raw_buffer_load_b128(rs, (kq * 4 + src) * 16384 + (2 * w + i) * 1024 + l * 16, 0, 16);
    }
#pragma unroll
    for (int i = 0; i < 2; ++i) {
      const f32x4 own = *(const f32x4*)(smem + (2 * w + i) * 1024 + l * 16);
      const f32x4 r = (__builtin_bit_cast(f32x4, pr[0][i]) + __builtin_bit_cast(f32x4, pr[1][i])) + (__builtin_bit_cast(f32x4, pr[2][i]) + own);
      const int mm = mt * 128 + (kq >> 1) * 64 + ((w & 1) * 2 + i) * 16 + c, nn = nt * 128 + (kq & 1) * 64 + (w >> 1) * 16 + g * 4;
      u32x2 pkd = {pk2(r[0], r[1]), pk2(r[2], r[3])};
      *(u32x2*)(out + ((size_t)mm * 2048 + br * 1024 + nn) * 2) = pkd;
    }
  }
  STAMPA(8);
  STAMP_FLUSH(ows);
}

extern "C" void kernel_launch(void* const* d_in, const int* in_sizes, int n_in, void* d_out, int out_size, void* d_ws, size_t ws_size,
                              hipStream_t stream) {
  k1_qproj_attn<<<256, 512, 0, stream>>>((const char*)d_in[0], (const char*)d_in[1], (const char*)d_in[2], (const char*)d_in[3], (const char*)d_in[5],
                                         (const char*)d_in[6], (const char*)d_in[7], (const char*)d_in[8], (char*)d_ws, (const char*)d_in[4],
                                         (const char*)d_in[9], (char*)d_out);
}
```

```cpp
#include <hip/hip_runtime.h>
#include <hip/hip_bf16.h>

#define POL_Q 2
#define POL_KVA 2

typedef short bf16x8 __attribute__((ext_vector_type(8)));
typedef short s16x4 __attribute__((ext_vector_type(4)));
typedef float f32x4 __attribute__((ext_vector_type(4)));
typedef float f32x2 __attribute__((ext_vector_type(2)));
typedef __bf16 bf16x2 __attribute__((ext_vector_type(2)));
typedef unsigned u32x4 __attribute__((ext_vector_type(4)));
typedef unsigned u32x2 __attribute__((ext_vector_type(2)));
#define LAS __attribute__((address_space(3)))
#define DI __device__ __forceinline__

#define WAIT_V(n) asm volatile("s_waitcnt vmcnt(%0)" ::"n"(n) : "memory")
#define WAIT_L0() do { asm volatile("" ::: "memory"); __builtin_amdgcn_s_waitcnt(0xC07F); asm volatile("" ::: "memory"); } while (0)
#define BAR() __builtin_amdgcn_s_barrier()
#define SB() __builtin_amdgcn_sched_barrier(0)

template <bool NT>
DI void glds(const char* sbase, unsigned voff, unsigned ldsaddr) {
  if (NT)
    asm volatile("s_mov_b32 m0, %2\n\ts_nop 0\n\tglobal_load_lds_dwordx4 %0, %1 nt" ::"v"(voff), "s"(sbase), "s"(ldsaddr) : "memory");
  else
    asm volatile("s_mov_b32 m0, %2\n\ts_nop 0\n\tglobal_load_lds_dwordx4 %0, %1" ::"v"(voff), "s"(sbase), "s"(ldsaddr) : "memory");
}
template <bool NT>
DI void glds4(const char* sbase, unsigned v0, unsigned v1, unsigned v2, unsigned v3, unsigned ldsaddr) {
  if (NT)
    asm volatile("s_mov_b32 m0, %5\n\ts_nop 0\n\t"
                 "global_load_lds_dwordx4 %0, %4 nt\n\t"
                 "global_load_lds_dwordx4 %1, %4 offset:1024 nt\n\t"
                 "global_load_lds_dwordx4 %2, %4 offset:2048 nt\n\t"
                 "global_load_lds_dwordx4 %3, %4 offset:3072 nt"
                 ::"v"(v0), "v"(v1), "v"(v2), "v"(v3), "s"(sbase), "s"(ldsaddr) : "memory");
  else
    asm volatile("s_mov_b32 m0, %5\n\ts_nop 0\n\t"
                 "global_load_lds_dwordx4 %0, %4\n\t"
                 "global_load_lds_dwordx4 %1, %4 offset:1024\n\t"
                 "global_load_lds_dwordx4 %2, %4 offset:2048\n\t"
                 "global_load_lds_dwordx4 %3, %4 offset:3072"
                 ::"v"(v0), "v"(v1), "v"(v2), "v"(v3), "s"(sbase), "s"(ldsaddr) : "memory");
}
DI void pin(const char* sbase, unsigned voff, unsigned ldsaddr) {
  asm volatile("s_mov_b32 m0, %2\n\ts_nop 0\n\tglobal_load_lds_dword %0, %1" ::"v"(voff), "s"(sbase), "s"(ldsaddr) : "memory");
}

DI f32x4 mfma16(bf16x8 a, bf16x8 b, f32x4 c) { return __builtin_amdgcn_mfma_f32_16x16x32_bf16(a, b, c, 0, 0, 0); }
DI unsigned pk2(float lo, float hi) { f32x2 a = {lo, hi}; return __builtin_bit_cast(unsigned, __builtin_convertvector(a, bf16x2)); }
DI bf16x8 lds_b128(const char* smem, unsigned off) { return *(const bf16x8*)(smem + off); }
DI s16x4 lds_tr(char* smem, unsigned off) { return __builtin_amdgcn_ds_read_tr16_b64_v4i16((LAS s16x4*)(smem + off)); }
DI bf16x8 cat8(s16x4 lo, s16x4 hi) { return __builtin_shufflevector(lo, hi, 0, 1, 2, 3, 4, 5, 6, 7); }
DI float xg_max(float x) {
  auto s = __builtin_amdgcn_permlane16_swap(__float_as_uint(x), __float_as_uint(x), false, false);
  x = fmaxf(__uint_as_float(s[0]), __uint_as_float(s[1]));
  auto t = __builtin_amdgcn_permlane32_swap(__float_as_uint(x), __float_as_uint(x), false, false);
  return fmaxf(__uint_as_float(t[0]), __uint_as_float(t[1]));
}
DI float xg_sum(float x) {
  auto s = __builtin_amdgcn_permlane16_swap(__float_as_uint(x), __float_as_uint(x), false, false);
  x = __uint_as_float(s[0]) + __uint_as_float(s[1]);
  auto t = __builtin_amdgcn_permlane32_swap(__float_as_uint(x), __float_as_uint(x), false, false);
  return __uint_as_float(t[0]) + __uint_as_float(t[1]);
}

#define STAMP(k)
#define STAMP_DECL
#define STAMP_FLUSH(ws)
constexpr float SL2E = 0.088388347648318447f * 1.4426950408889634f;

constexpr size_t PART_OFF = 16u << 20, CNT_OFF = 8u << 20, Q_OFF = 10u << 20;
#define ASLEEP 4

#define G8_(j, off, sfx) "global_load_lds_dwordx4 %" #j ", %8 offset:" #off sfx "\n\t"
template <int MODE>
DI void glds8(const char* sbase, const unsigned (&v)[8], unsigned ldsaddr) {
  if (MODE == 0)
    asm volatile("s_mov_b32 m0, %9\n\ts_nop 0\n\t" G8_(0, -4096, "") G8_(1, -3072, "") G8_(2, -2048, "") G8_(3, -1024, "") G8_(4, 0, "") G8_(5, 1024, "") G8_(6, 2048, "") G8_(7, 3072, "")
                 ::"v"(v[0]), "v"(v[1]), "v"(v[2]), "v"(v[3]), "v"(v[4]), "v"(v[5]), "v"(v[6]), "v"(v[7]), "s"(sbase), "s"(ldsaddr) : "memory");
  else if (MODE == 3)
    asm volatile("s_mov_b32 m0, %9\n\ts_nop 0\n\t" G8_(0, -4096, " sc1") G8_(1, -3072, " sc1") G8_(2, -2048, " sc1") G8_(3, -1024, " sc1") G8_(4, 0, " sc1") G8_(5, 1024, " sc1") G8_(6, 2048, " sc1") G8_(7, 3072, " sc1")
                 ::"v"(v[0]), "v"(v[1]), "v"(v[2]), "v"(v[3]), "v"(v[4]), "v"(v[5]), "v"(v[6]), "v"(v[7]), "s"(sbase), "s"(ldsaddr) : "memory");
  else if (MODE == 1)
    asm volatile("s_mov_b32 m0, %9\n\ts_nop 0\n\t" G8_(0, -4096, " nt") G8_(1, -3072, " nt") G8_(2, -2048, " nt") G8_(3, -1024, " nt") G8_(4, 0, " nt") G8_(5, 1024, " nt") G8_(6, 2048, " nt") G8_(7, 3072, " nt")
                 ::"v"(v[0]), "v"(v[1]), "v"(v[2]), "v"(v[3]), "v"(v[4]), "v"(v[5]), "v"(v[6]), "v"(v[7]), "s"(sbase), "s"(ldsaddr) : "memory");
  else
    asm volatile("s_mov_b32 m0, %9\n\ts_nop 0\n\t" G8_(0, -4096, " nt") G8_(0, -4096, "") G8_(1, -3072, " nt") G8_(1, -3072, "") G8_(2, -2048, " nt") G8_(2, -2048, "") G8_(3, -1024, " nt") G8_(3, -1024, "")
                 G8_(4, 0, " nt") G8_(4, 0, "") G8_(5, 1024, " nt") G8_(5, 1024, "") G8_(6, 2048, " nt") G8_(6, 2048, "") G8_(7, 3072, " nt") G8_(7, 3072, "")
                 ::"v"(v[0]), "v"(v[1]), "v"(v[2]), "v"(v[3]), "v"(v[4]), "v"(v[5]), "v"(v[6]), "v"(v[7]), "s"(sbase), "s"(ldsaddr) : "memory");
}
#define CVAR 1
#define RPG 2
#define H0SLEEP 12
#define KVAPOL 0
#define BLKBAR 0
#define BLKBAR2 0
#define BNT 1
#define FNT 0
#define WOPF 0
#define NT_VB 1
#define NT_KB 1
#define NT_A 1
#define TLBW 0
#define OAGATE 0
#define PRIO 0
#define TROT 1
#define QPF 1
#define F1LATE 1
#define XPF 0
#define FDUP 1
#define GSARG
#define GSPASS
#define GSPASS2
#define GBAR() BAR()
#define GST(k)
#define RLX_AGENT __ATOMIC_RELAXED, __HIP_MEMORY_SCOPE_AGENT
DI void poll_ge(unsigned* p, unsigned want) {
  unsigned spins = 0;
  while (__hip_atomic_load(p, RLX_AGENT) < want && ++spins < (1u << 22)) __builtin_amdgcn_s_sleep(1);
  asm volatile("" ::: "memory");
}
DI void tile_ld(u32x4 (&r)[4], const char* sbase, unsigned v0, unsigned v1, unsigned v2, unsigned v3) {
  asm volatile("global_load_dwordx4 %0, %4, %8 nt\n\tglobal_load_dwordx4 %1, %5, %8 offset:1024 nt\n\t"
               "global_load_dwordx4 %2, %6, %8 offset:2048 nt\n\tglobal_load_dwordx4 %3, %7, %8 offset:3072 nt"
               : "=&v"(r[0]), "=&v"(r[1]), "=&v"(r[2]), "=&v"(r[3]) : "v"(v0), "v"(v1), "v"(v2), "v"(v3), "s"(sbase) : "memory");
}
DI void glds_wx(const char* wbase, const char* xbase, const unsigned (&v)[8], unsigned ldsaddr) {
#define GW_(j, off) "global_load_lds_dwordx4 %" #j ", %8 offset:" #off "\n\t"
#define GX_(j, off) "global_load_lds_dwordx4 %" #j ", %9 offset:" #off "\n\t"
  asm volatile("s_mov_b32 m0, %10\n\ts_nop 0\n\t" GW_(0, -4096) GW_(1, -3072) GW_(2, -2048) GW_(3, -1024) GX_(4, 0) GX_(5, 1024) GX_(6, 2048) GX_(7, 3072)
               ::"v"(v[0]), "v"(v[1]), "v"(v[2]), "v"(v[3]), "v"(v[4]), "v"(v[5]), "v"(v[6]), "v"(v[7]), "s"(wbase), "s"(xbase), "s"(ldsaddr) : "memory");
}
struct HFrags { s16x4 a[4][2]; bf16x8 b[4]; };
template <int WS, int XT, bool TAIL, class TailF, class LandF, class DumpF>
DI void gemm_core(char* smem, unsigned lbase, const char* wg, const char* xg, int rot, int w, int l, f32x4 (&acc)[4][4], TailF tail, LandF land, DumpF dump, bool dotail, unsigned* gate GSARG) {
  int bn_ = XT ? 21 : 1; (void)bn_;
  const int c = l & 15, g = l >> 4, q4 = c >> 2, p = l & 3;
  if (w >= 4) {
    u32x4 lr[10][4];
    if (TAIL && !dotail) {
#pragma unroll
      for (int k = 0; k < 10; ++k) land(k, lr[k]);
      WAIT_V(0);
    }
    const int iw = w & 1;
    unsigned vw[8], vx[8];
    {
      const unsigned vw0 = (unsigned)(g * WS + ((c ^ (2 * g)) * 16)), vw1 = vw0 ^ 128;
      const unsigned vx0 = XT ? (unsigned)(l * 16) : (unsigned)((l >> 3) * 2048 + (((l & 7) ^ g) * 16));
      const unsigned vx1 = XT ? vx0 : (vx0 ^ 64);
#pragma unroll
      for (int j = 0; j < 8; ++j) {
        vw[j] = ((j & 2) ? vw1 : vw0) + j * (4 * WS - 1024);
        vx[j] = XT ? vx0 + (j >> 1) * (128 * 1024 - 2048) : ((j & 1) ? vx1 : vx0) + j * (16384 - 1024);
      }
    }
    const char* w_w = wg + (size_t)(32 * iw) * WS + 4096;
    const char* x_w = (XT ? xg + (size_t)(4 * iw) * (128 * 1024) : xg + (size_t)(64 * iw) * 2048) + 4096;
    const unsigned lw0 = lbase + iw * 8192 + 4096;
    auto wpieces = [&](int kap, unsigned bufoff, bool first) {
      if (first && FNT) glds8<FDUP ? 2 : 1>(w_w + (size_t)kap * (64 * WS), vw, lw0 + bufoff);
      else glds8<0>(w_w + (size_t)kap * (64 * WS), vw, lw0 + bufoff);
    };
    auto xpieces = [&](int kap, unsigned bufoff) { glds8<XT ? 3 : 0>(x_w + kap * (XT ? 2048 : 128), vx, lw0 + bufoff + 16384); };
    constexpr int FN = (FDUP && FNT) ? 24 : 16;
    if (w < 6) {
      if (XT) {
#pragma unroll 1
        for (int f = 0; f < 2; ++f) wpieces(rot + f, f * 32768, true);
        if (w == 4) poll_ge(gate, 8u);
        BAR();
        GST(20);
#pragma unroll 1
        for (int f = 0; f < 2; ++f) xpieces(rot + f, f * 32768);
        WAIT_V(8);
      } else if (F1LATE) {
        wpieces(rot, 0, true); xpieces(rot, 0);
        WAIT_V(0);
      } else {
#pragma unroll 1
        for (int f = 0; f < 2; ++f) { wpieces(rot + f, f * 32768, true); xpieces(rot + f, f * 32768); }
      }
#pragma unroll 1
      for (int i = 0; i < 2; ++i) {
        WAIT_V(FN);
        GBAR();
        if (F1LATE && !XT && i == 0) { wpieces(rot + 1, 32768, true); xpieces(rot + 1, 32768); }
        GBAR();
        wpieces(rot + i + 2, i * 32768, true); xpieces(rot + i + 2, i * 32768);
        GBAR();
        GBAR();
      }
      WAIT_V(FN);
      GBAR();
      GBAR();
      if (TAIL && dotail) tail(0);
      GBAR();
      if (TAIL && dotail) tail(1);
      GBAR();
      if (TAIL && dotail) { tail(2); WAIT_V(12); } else WAIT_V(0);
      GBAR();
      if (TAIL && dotail) tail(3);
      GBAR();
      GBAR();
      GBAR();
    } else {
      auto stage = [&](int kap, int hb) { wpieces(kap & 15, 65536 + hb * 32768, false); xpieces(kap & 15, 65536 + hb * 32768); };
      __builtin_amdgcn_s_sleep(H0SLEEP);
      if (XT) { wpieces((rot + 4) & 15, 65536, false); BAR(); GST(20); xpieces((rot + 4) & 15, 65536); }
      else stage(rot + 4, 0);
      GBAR();
      stage(rot + 8, 1);
      WAIT_V(16);
      GBAR();
      stage(rot + 12, 2);
#pragma unroll 1
      for (int i = 0; i < 3; ++i) {
        WAIT_V(16);
        GBAR();
        stage(rot + 5 + i, 0);
        WAIT_V(16);
        GBAR();
        stage(rot + 9 + i, 1);
        GBAR();
        stage(rot + 13 + i, 2);
        WAIT_V(32);
        GBAR();
      }
      if (TAIL && dotail) { tail(0); tail(1); WAIT_V(24); } else WAIT_V(16);
      GBAR();
      if (TAIL && dotail) { tail(2); tail(3); WAIT_V(16); } else WAIT_V(0);
      GBAR();
    }
    if (TAIL && !dotail) {
#pragma unroll
      for (int k = 0; k < 8; ++k) dump(k, lr[k]);
      BAR();
      dump(8, lr[8]); dump(9, lr[9]);
      asm volatile("s_waitcnt lgkmcnt(0)" ::: "memory");
    }
  } else {
    if (XT) {
      BAR(); GST(20);
      if (XPF) {
        unsigned vp[8];
#pragma unroll
        for (int j = 0; j < 8; ++j) vp[j] = l * 16 + (j >> 2) * (128 * 1024 - 4096);
        glds8<3>(xg + (size_t)(2 * w) * (128 * 1024) + (rot + 2) * 2048 + 4096, vp, lbase + 131072 + w * 8192 + 4096);
        WAIT_V(0);
      }
    }
    const int nh = w & 1, mh = w >> 1;
    unsigned aw[4];
    {
      const int F = 2 * q4 | 8 * (g & 1);
#pragma unroll
      for (int t = 0; t < 4; ++t) aw[t] = (unsigned)((8 * g + q4) * 256 + ((((2 * (4 * nh + t)) ^ F) + (p >> 1)) * 16) + (p & 1) * 8);
    }
    const unsigned ax0 = XT ? (unsigned)(16384 + mh * 8192 + l * 16) : (unsigned)(16384 + (64 * mh + c) * 128 + ((g ^ (c >> 1)) * 16));
    const unsigned ax1 = XT ? ax0 + 1024 : (ax0 ^ 64);
    struct FF { HFrags h[2]; };
    auto mm = [&](const FF& fo) {
#pragma unroll
      for (int ks = 0; ks < 2; ++ks)
#pragma unroll
        for (int t = 0; t < 4; ++t)
#pragma unroll
          for (int v = 0; v < 4; ++v) acc[t][v] = mfma16(cat8(fo.h[ks].a[t][0], fo.h[ks].a[t][1]), fo.h[ks].b[v], acc[t][v]);
    };
    auto full = [&](FF& fn, const FF& fo, unsigned bo) {
#pragma unroll
      for (int ks = 0; ks < 2; ++ks)
#pragma unroll
        for (int t = 0; t < 4; ++t) {
          fn.h[ks].b[t] = lds_b128(smem, (ks ? ax1 : ax0) + bo + t * 2048);
          fn.h[ks].a[t][0] = lds_tr(smem, aw[t] + bo + ks * 8192);
          fn.h[ks].a[t][1] = lds_tr(smem, aw[t] + bo + ks * 8192 + 1024);
        }
      mm(fo);
#pragma unroll
      for (int t = 0; t < 24; ++t) {
        __builtin_amdgcn_sched_group_barrier(0x008, 1, 0);
        __builtin_amdgcn_sched_group_barrier(0x100, 1, 0);
      }
      __builtin_amdgcn_sched_group_barrier(0x008, 8, 0);
      SB();
    };
#pragma unroll
    for (int t = 0; t < 4; ++t)
#pragma unroll
      for (int v = 0; v < 4; ++v) acc[t][v] = f32x4{0, 0, 0, 0};
    FF f0, f1;
#pragma unroll
    for (int ks = 0; ks < 2; ++ks)
#pragma unroll
      for (int t = 0; t < 4; ++t) { f1.h[ks].b[t] = bf16x8{0, 0, 0, 0, 0, 0, 0, 0}; f1.h[ks].a[t][0] = s16x4{0, 0, 0, 0}; f1.h[ks].a[t][1] = s16x4{0, 0, 0, 0}; }
#pragma unroll 1
    for (int n = 0; n < 16; n += 2) {
      const unsigned bo0 = (n & 2) ? 98304 : ((n >> 2) & 1) * 32768, bo1 = (n & 2) ? 131072 : 65536;
      WAIT_L0();
      GBAR();
      full(f0, f1, bo0);
      WAIT_L0();
      GBAR();
      full(f1, f0, bo1);
    }
    WAIT_L0();
    if (TAIL && !dotail) BAR();
    mm(f1);
  }
}

__global__ __launch_bounds__(512, 2) void k1_qproj_attn(const char* __restrict__ xA, const char* __restrict__ kA, const char* __restrict__ vA,
                                                        const char* __restrict__ wqA, const char* __restrict__ xB, const char* __restrict__ kB,
                                                        const char* __restrict__ vB, const char* __restrict__ wqB, char* __restrict__ ows,
                                                        const char* __restrict__ woA, const char* __restrict__ woB, char* __restrict__ out) {
  __shared__ __attribute__((aligned(1024))) char smem[163840];
  const unsigned lbase = (unsigned)(size_t)(LAS char*)smem;
  const int tid = threadIdx.x, l = tid & 63, w = __builtin_amdgcn_readfirstlane(tid >> 6);
  const int xcd = blockIdx.x & 7, j = blockIdx.x >> 3;
  const int br = j & 1, idx = j >> 1;
  const int bg = idx & 3, qhead = 4 * xcd + (idx >> 2), rot = 4 * (((idx >> 2) + bg) & 3);
  const char* xg = (br ? xB : xA) + (size_t)bg * 128 * 2048;
  const char* wq = (br ? wqB : wqA) + qhead * 256;
  const int c = l & 15, g = l >> 4, q4 = c >> 2, p = l & 3;
  const int u = w & 3, dh = w >> 2;
  const int b = br ? 4 * bg + u : (xcd & 1) * 8 + (idx >> 1);
  const int head = br ? qhead : 8 * (xcd >> 1) + 4 * (idx & 1) + u;
  const unsigned ring = (unsigned)((w >= 4 ? w - 4 : w < 2 ? w + 4 : w + 6) * 16384);
  STAMP_DECL;
  STAMP(0);
  if (QPF && !br && w < 4) {
    unsigned vp[8];
#pragma unroll
    for (int i = 0; i < 4; ++i) {
      vp[i] = (unsigned)(g * 8192 + c * 16 + i * (4 * 8192 - 1024));
      vp[4 + i] = (unsigned)((l >> 3) * 2048 + (l & 7) * 16 + i * (16384 - 1024));
    }
    const char* wb = wqB + qhead * 256 + (size_t)(16 * w) * 8192 + 4096;
    const char* xb = xB + (size_t)(bg * 128 + 32 * w) * 2048;
#pragma unroll 1
    for (int k = 1; k < 4; ++k) glds_wx(wb + (size_t)(rot + k) * (64 * 8192), xb + (rot + k) * 128, vp, lbase + 131072 + w * 8192 + 4096);
    WAIT_V(0);
  }
  if (ASLEEP && !br) {
#pragma unroll 1
    for (int i = 0; i < ASLEEP; ++i) __builtin_amdgcn_s_sleep(16);
  }
  const size_t kvoff = ((size_t)(br ? b * 32 + head : b * 4 + (xcd >> 1)) * 512 + dh * 256) * 256;
  const char* kg = (br ? kB : kA) + kvoff;
  const char* vg = (br ? vB : vA) + kvoff;
  unsigned vk[4], vv[2];
#pragma unroll
  for (int i = 0; i < 4; ++i) vk[i] = (unsigned)(g * 256 + ((c ^ g ^ (4 * i)) * 16));
#pragma unroll
  for (int i = 0; i < 2; ++i) vv[i] = (unsigned)(g * 256 + ((c ^ (2 * g) ^ (8 * i)) * 16));
  const int r16 = TROT ? (br ? 2 * w + idx : idx + xcd * 2) : 0;
  auto slot_off = [](int t) constexpr { return t * 4096; };
  auto tile_to = [&](int blk, int isV, int t, unsigned base) {
    const unsigned la = lbase + base + t * 4096;
    const char* src = (isV ? vg : kg) + ((blk * 4 + t + r16) & 15) * 4096;
    const bool nt = br ? (isV ? NT_VB : NT_KB) : NT_A;
    if (nt) {
      if (isV) glds4<true>(src, vv[0], vv[1], vv[0], vv[1], la);
      else glds4<true>(src, vk[0], vk[1], vk[2], vk[3], la);
    } else {
      if (isV) glds4<false>(src, vv[0], vv[1], vv[0], vv[1], la);
      else glds4<false>(src, vk[0], vk[1], vk[2], vk[3], la);
    }
  };
  auto tile_issue = [&](int blk, int isV, int t) { tile_to(blk, isV, t, ring); };

  unsigned* ctl = (unsigned*)(ows + CNT_OFF);
  const __amdgpu_buffer_rsrc_t rc = __builtin_amdgcn_make_buffer_rsrc(ows + CNT_OFF, 0, 65536 + 16384, 0x00020000);
  unsigned nonce = 0;
  auto echo = [&]() {
    const u32x4 r = __builtin_amdgcn_raw_buffer_load_b128(rc, l * 16, 0, 16);
    __builtin_amdgcn_raw_buffer_store_b128(r, rc, 4096 + l * 16, 0, 16);
  };
  if (w == 0) {
    nonce = __hip_atomic_load(ctl + 1024 + blockIdx.x, RLX_AGENT) + 1u;
    if (l == 0) __hip_atomic_store(ctl + blockIdx.x, nonce, RLX_AGENT);
  } else if (w == 1 && blockIdx.x == 0) {
#pragma unroll
    for (int i = 0; i < 6; ++i) __builtin_amdgcn_raw_buffer_store_b32(0u, rc, 8192 + (i * 64 + l) * 128, 0, 16);
    WAIT_V(0);
    __builtin_amdgcn_s_sleep(24);
    echo();
  }
  if (TLBW && w == 2) {
    const int kq_ = xcd >> 1;
    const char* a;
    const int uu = l & 3;
    const size_t kvo = ((size_t)(br ? (4 * bg + uu) * 32 + qhead : b * 4 + kq_) * 512) * 256;
    if (l < 4) a = (br ? kB : kA) + kvo;
    else if (l < 8) a = (br ? vB : vA) + kvo;
    else if (l < 10) a = (br ? woB : woA) + (size_t)(kq_ * 1024 + (l & 1) * 1023) * 2048;
    else if (l < 12) a = ows + (size_t)br * (4u << 20) + (l & 1) * (2u << 20) + (12u << 10);
    else if (l < 14) a = ows + Q_OFF + (l & 1) * (2u << 20);
    else if (l < 15) a = ows + PART_OFF + (size_t)(br * 32 + ((xcd & 1) * 4 + (idx >> 2)) * 4 + (idx & 3)) * (16 * 16384);
    else a = out;
    if (l < 16) { unsigned t_ = __builtin_nontemporal_load((const unsigned*)a); asm volatile("" ::"v"(t_)); }
  }
  const int kq = xcd >> 1, mt = idx & 3, nl = idx >> 2, nt = (xcd & 1) * 4 + nl;
  const __amdgpu_buffer_rsrc_t rs = __builtin_amdgcn_make_buffer_rsrc(ows + PART_OFF + (size_t)(br * 32 + nt * 4 + mt) * (16 * 8192), 0, 16 * 8192, 0x00020000);
  if (!br && w >= 1 && w < 4) {
    const int src = (w - 1) + ((w - 1) >= kq);
    const __amdgpu_buffer_rsrc_t r2 = __builtin_amdgcn_make_buffer_rsrc(ows + PART_OFF + (size_t)(nt * 4 + mt) * (16 * 8192), 0, 33 * 16 * 8192, 0x00020000);
#pragma unroll
    for (int i = 0; i < 16; ++i)
      __builtin_amdgcn_raw_buffer_store_b128(u32x4{~0u, ~0u, ~0u, ~0u}, r2, (i >> 3) * (32 * 16 * 8192) + (kq * 4 + src) * 8192 + (i & 7) * 1024 + l * 16, 0, 16);
    WAIT_V(0);
  }
  unsigned* cnt = ctl + 2048 + 32;
  auto confirm = [&]() {
    unsigned spins = 0;
    while (__hip_atomic_load(ctl + 1024 + blockIdx.x, RLX_AGENT) != nonce && ++spins < (1u << 18)) __builtin_amdgcn_s_sleep(2);
    if (l == 0) __hip_atomic_fetch_add(ctl + 2048, 1u, RLX_AGENT);
  };
  f32x4 qa[4][4];
  auto land = [&](int k, u32x4 (&r)[4]) {
    const int ph = k >> 1, hf = k & 1;
    const char* src = ((ph & 1) ? vg : kg) + (hf - 1) * 65536 + (((ph >> 1) * 4 + u + r16) & 15) * 4096;
    if (ph & 1) tile_ld(r, src, vv[0], vv[1], vv[0], vv[1]);
    else tile_ld(r, src, vk[0], vk[1], vk[2], vk[3]);
  };
  auto dump = [&](int k, const u32x4 (&r)[4]) {
    const unsigned d = k * 16384 + u * 4096 + l * 16;
    asm volatile("ds_write_b128 %0, %1\n\tds_write_b128 %0, %2 offset:1024\n\tds_write_b128 %0, %3 offset:2048\n\tds_write_b128 %0, %4 offset:3072"
                 ::"v"(lbase + d), "v"(r[0]), "v"(r[1]), "v"(r[2]), "v"(r[3]) : "memory");
  };
  gemm_core<8192, 0, true>(smem, lbase, wq, xg, rot, w, l, qa, [&](int n) { tile_issue(0, 0, n); }, land, dump, br != 0, nullptr GSPASS);
  bf16x8 qf[2][4];
  if (br) {
    if (w < 4) {
#pragma unroll
      for (int t = 0; t < 4; ++t)
#pragma unroll
        for (int v = 0; v < 4; ++v) {
          const int tau = 4 * (w & 1) + t;
          u32x2 pkd = {pk2(qa[t][v][0], qa[t][v][1]), pk2(qa[t][v][2], qa[t][v][3])};
          const unsigned off = 98304 + ((w >> 1) * 4 + v) * 4096 + (tau >> 1) * 1024 + ((2 * (tau & 1) + (g >> 1)) * 16 + c) * 16 + (g & 1) * 8;
          *(u32x2*)(smem + off) = pkd;
        }
    }
    WAIT_L0();
    BAR();
#pragma unroll
    for (int v = 0; v < 2; ++v)
#pragma unroll
      for (int s = 0; s < 4; ++s) qf[v][s] = lds_b128(smem, 98304 + u * 8192 + v * 4096 + s * 1024 + l * 16);
    if (w == 0) confirm();
    if (w < 4) {
#pragma unroll 1
      for (int t = 0; t < 4; ++t) tile_issue(0, 0, t);
    }
  } else {
    const __amdgpu_buffer_rsrc_t rq = __builtin_amdgcn_make_buffer_rsrc(ows + Q_OFF, 0, 4u << 20, 0x00020000);
    if (w < 4) {
      const int qo = ((bg * 8 + (w >> 1) * 4) * 128 + qhead * 4 + (w & 1) * 2) * 1024 + ((g >> 1) * 16 + c) * 16 + (g & 1) * 8;
#pragma unroll
      for (int t = 0; t < 4; ++t)
#pragma unroll
        for (int v = 0; v < 4; ++v) {
          u32x2 pkd = {pk2(qa[t][v][0], qa[t][v][1]), pk2(qa[t][v][2], qa[t][v][3])};
          __builtin_amdgcn_raw_buffer_store_b64(pkd, rq, qo + v * (128 * 1024) + t * 512, 0, 16);
        }
      if (w == 1 && blockIdx.x == 0) echo();
      if (w == 0) confirm();
      WAIT_V(0);
    }
    BAR();
    if (tid == 0) __hip_atomic_fetch_add(cnt + (288 + xcd * 4 + bg) * 32, 1u, RLX_AGENT);
    if (w == 1 && blockIdx.x == 0) {
      unsigned spins = 0;
      while (__hip_atomic_load(ctl + 2048, RLX_AGENT) < 256u && ++spins < (1u << 17)) { echo(); __builtin_amdgcn_s_sleep(8); }
    }
    if (w == 4) poll_ge(cnt + (288 + (head >> 2) * 4 + (b >> 2)) * 32, 4u);
    BAR();
#pragma unroll
    for (int v = 0; v < 2; ++v)
#pragma unroll
      for (int s = 0; s < 4; ++s)
        qf[v][s] = __builtin_bit_cast(bf16x8, __builtin_amdgcn_raw_buffer_load_b128(rq, ((2 * b + v) * 128 + head * 4 + s) * 1024 + l * 16, 0, 16));
    WAIT_V(0);
  }
  STAMP(1);
#define STAMPA(k) STAMP(k)

  unsigned ak[4], av[8];
#pragma unroll
  for (int s = 0; s < 4; ++s) ak[s] = (unsigned)(c * 256 + ((g ^ c ^ (4 * s)) * 16));
  {
    const int row = 4 * g + q4, F = 2 * (row & 7);
#pragma unroll
    for (int t = 0; t < 8; ++t) av[t] = (unsigned)(row * 256 + ((((2 * t) ^ F) + (p >> 1)) * 16) + (p & 1) * 8);
  }
  f32x4 o[8][2];
#pragma unroll
  for (int t = 0; t < 8; ++t) { o[t][0] = f32x4{0, 0, 0, 0}; o[t][1] = f32x4{0, 0, 0, 0}; }
  unsigned tb = ring, pnext = dh * 16384u, pfree = 4 * 32768u + dh * 16384u;
  float m[2] = {-__builtin_inff(), -__builtin_inff()}, ls[2] = {0.f, 0.f};
#pragma unroll 1
  for (int blk = 0; blk < 4; ++blk) {
    f32x4 sc[4][2];
    if (PRIO && br) {
      if (PRIO == 1) { if (blk == 0) __builtin_amdgcn_s_setprio(3); else if (blk == 1) __builtin_amdgcn_s_setprio(2); else if (blk == 2) __builtin_amdgcn_s_setprio(1); else __builtin_amdgcn_s_setprio(0); }
      else { if (blk == 0) __builtin_amdgcn_s_setprio(3); else if (blk == 2) __builtin_amdgcn_s_setprio(2); else if (blk == 3) __builtin_amdgcn_s_setprio(0); }
    }
    if (!br) {
      if (blk < 3) WAIT_V(12); else WAIT_V(4);
      BAR();
      if (blk == 1) tile_to(blk + 2, 0, u, pfree);
      pfree = tb; tb = pnext; pnext = pnext >= 4 * 32768u ? pnext - 4 * 32768u : pnext + 32768u;
    }
#pragma unroll
    for (int t = 0; t < 4; ++t) {
      if (br) WAIT_V(12);
      bf16x8 kf[4];
#pragma unroll
      for (int s = 0; s < 4; ++s) kf[s] = lds_b128(smem, ak[s] + tb + slot_off(t));
      WAIT_L0();
      if (br) tile_issue(blk, 1, t);
      sc[t][0] = f32x4{0, 0, 0, 0}; sc[t][1] = f32x4{0, 0, 0, 0};
#pragma unroll
      for (int s = 0; s < 4; ++s)
#pragma unroll
        for (int v = 0; v < 2; ++v) sc[t][v] = mfma16(kf[s], qf[v][s], sc[t][v]);
      SB();
    }
    bf16x8 pf[2][2];
#pragma unroll
    for (int v = 0; v < 2; ++v) {
      float mx = sc[0][v][0];
#pragma unroll
      for (int t = 0; t < 4; ++t)
#pragma unroll
        for (int i = 0; i < 4; ++i) mx = fmaxf(mx, sc[t][v][i]);
      mx = xg_max(mx) * SL2E;
      const float mn = fmaxf(m[v], mx);
      const float alpha = __builtin_amdgcn_exp2f(m[v] - mn);
      ls[v] *= alpha;
#pragma unroll
      for (int t = 0; t < 8; ++t) o[t][v] *= alpha;
      m[v] = mn;
      float sum = 0.f;
#pragma unroll
      for (int t = 0; t < 4; ++t)
#pragma unroll
        for (int i = 0; i < 4; ++i) { const float e = __builtin_amdgcn_exp2f(fmaf(sc[t][v][i], SL2E, -mn)); sc[t][v][i] = e; sum += e; }
      ls[v] += sum;
#pragma unroll
      for (int kb = 0; kb < 2; ++kb) {
        u32x4 pw = {pk2(sc[2 * kb][v][0], sc[2 * kb][v][1]), pk2(sc[2 * kb][v][2], sc[2 * kb][v][3]),
                    pk2(sc[2 * kb + 1][v][0], sc[2 * kb + 1][v][1]), pk2(sc[2 * kb + 1][v][2], sc[2 * kb + 1][v][3])};
        pf[kb][v] = __builtin_bit_cast(bf16x8, pw);
      }
    }
    const bool more = blk < 3;
    if (PRIO == 2 && br && blk == 2) __builtin_amdgcn_s_setprio(1);
    if (!br) {
      if (blk < 2) WAIT_V(12); else if (blk == 2) WAIT_V(8); else WAIT_V(0);
      BAR();
      pfree = tb;
      if (blk < 2) tile_to(blk + 2, 1, u, pfree);
      tb = pnext; pnext = pnext >= 4 * 32768u ? pnext - 4 * 32768u : pnext + 32768u;
      pfree = tb;
    }
#pragma unroll
    for (int kb = 0; kb < 2; ++kb) {
      s16x4 vlo[8], vhi[8];
      if (br) { if (kb == 0 || more) WAIT_V(12); else WAIT_V(4); }
#pragma unroll
      for (int t = 0; t < 8; ++t) vlo[t] = lds_tr(smem, av[t] + tb + slot_off(2 * kb));
      if (br) {
        WAIT_L0();
        if (more) { tile_issue(blk + 1, 0, 2 * kb); WAIT_V(12); }
        else if (kb == 0) WAIT_V(8);
        else WAIT_V(0);
      }
#pragma unroll
      for (int t = 0; t < 8; ++t) vhi[t] = lds_tr(smem, av[t] + tb + slot_off(2 * kb + 1));
      WAIT_L0();
      if (br && more) tile_issue(blk + 1, 0, 2 * kb + 1);
#pragma unroll
      for (int t = 0; t < 8; ++t)
#pragma unroll
        for (int v = 0; v < 2; ++v) o[t][v] = mfma16(cat8(vlo[t], vhi[t]), pf[kb][v], o[t][v]);
      SB();
    }
    STAMPA(2 + blk);
  }
  if (!br) BAR();
  ls[0] = xg_sum(ls[0]);
  ls[1] = xg_sum(ls[1]);
  if (dh) {
#pragma unroll
    for (int t = 0; t < 8; ++t) { const f32x4 tmp = o[t][0]; o[t][0] = o[t][1]; o[t][1] = tmp; }
    float tm = m[0]; m[0] = m[1]; m[1] = tm;
    tm = ls[0]; ls[0] = ls[1]; ls[1] = tm;
  }
  {
#pragma unroll
    for (int t = 0; t < 8; ++t) *(f32x4*)(smem + ring + t * 1024 + l * 16) = o[t][1];
    f32x2 ml = {m[1], ls[1]};
    *(f32x2*)(smem + ring + 8192 + l * 8) = ml;
  }
  WAIT_L0();
  BAR();
  {
    const int pw = w ^ 4;
    const unsigned pring = (unsigned)((pw >= 4 ? pw - 4 : pw < 2 ? pw + 4 : pw + 6) * 16384);
    const f32x2 ml = *(const f32x2*)(smem + pring + 8192 + l * 8);
    const float mo = m[0], lo = ls[0];
    const float mm = fmaxf(mo, ml[0]);
    float a0 = __builtin_amdgcn_exp2f(mo - mm), a1 = __builtin_amdgcn_exp2f(ml[0] - mm);
    const float inv = 1.0f / (lo * a0 + ml[1] * a1);
    a0 *= inv; a1 *= inv;
    const __amdgpu_buffer_rsrc_t ro = __builtin_amdgcn_make_buffer_rsrc(ows + (size_t)br * (4u << 20) + ((size_t)(2 * b + dh) * 128 + head * 4) * 1024, 0, 4096, 0x00020000);
#pragma unroll
    for (int t = 0; t < 8; ++t) {
      const f32x4 oth = *(const f32x4*)(smem + pring + t * 1024 + l * 16);
      const f32x4 r = o[t][0] * a0 + oth * a1;
      u32x2 pkd = {pk2(r[0], r[1]), pk2(r[2], r[3])};
      __builtin_amdgcn_raw_buffer_store_b64(pkd, ro, t * 512 + ((g >> 1) * 16 + c) * 16 + (g & 1) * 8, 0, 16);
    }
  }
  WAIT_V(0);
  WAIT_L0();
  BAR();
  if (tid == 0) __hip_atomic_fetch_add(cnt + ((br * 4 + (b >> 2)) * 4 + (head >> 3)) * 32, 1u, RLX_AGENT);
  STAMPA(6);

  {
    if (w == 5) poll_ge(ctl + 2048, 256u);
    if (WOPF && !br && w < 4) {
      unsigned vp[8];
#pragma unroll
      for (int i = 0; i < 8; ++i) vp[i] = l * 16 + i * 1024;
      const char* src = woB + (size_t)(kq * 1024 + idx * 64 + w * 16) * 2048 + (xcd & 1) * 1024 + 4096;
      glds8<0>(src, vp, lbase + 98304 + w * 16384 + 4096);
      glds8<0>(src + 8 * 2048, vp, lbase + 98304 + w * 16384 + 8192 + 4096);
      WAIT_V(0);
    }
    if (OAGATE && !br) {
      if (w == 4) poll_ge(cnt + ((4 + mt) * 4 + kq) * 32, OAGATE);
      BAR();
    }
    const char* og = ows + (size_t)br * (4u << 20) + ((size_t)(mt * 8) * 128 + kq * 32) * 1024;
    const char* wo = (br ? woB : woA) + (size_t)(kq * 1024) * 2048 + nt * 256;
    f32x4 acc[4][4];
    gemm_core<2048, 1, false>(smem, lbase, wo, og, 4 * ((mt + nl) & 3), w, l, acc, [](int) {}, [](int, u32x4 (&)[4]) {}, [](int, const u32x4 (&)[4]) {}, false, cnt + ((br * 4 + mt) * 4 + kq) * 32 GSPASS2);
    STAMPA(7);
    if (w < 4) {
      if (w != kq) {
        const int off = (w * 4 + kq) * 8192 + l * 16;
#pragma unroll
        for (int t = 0; t < 4; ++t)
#pragma unroll
          for (int h = 0; h < 2; ++h) {
            u32x4 pk = {pk2(acc[t][2 * h][0], acc[t][2 * h][1]), pk2(acc[t][2 * h][2], acc[t][2 * h][3]),
                        pk2(acc[t][2 * h + 1][0], acc[t][2 * h + 1][1]), pk2(acc[t][2 * h + 1][2], acc[t][2 * h + 1][3])};
#pragma unroll
            for (int i = 0; i < 4; ++i) pk[i] = min(pk[i], 0xFFFFFFFEu);
            __builtin_amdgcn_raw_buffer_store_b128(pk, rs, off + (t * 2 + h) * 1024, 0, 16);
          }
      } else {
#pragma unroll
        for (int t = 0; t < 4; ++t)
#pragma unroll
          for (int v = 0; v < 4; ++v) *(f32x4*)(smem + (t * 4 + v) * 1024 + l * 16) = acc[t][v];
        WAIT_L0();
      }
    }
    GST(38);
    BAR();
    u32x4 pr[3];
    for (unsigned spins = 0; spins < (1u << 18); ++spins) {
      bool ok = true;
#pragma unroll
      for (int s = 0; s < 3; ++s) {
        pr[s] = __builtin_amdgcn_raw_buffer_load_b128(rs, (kq * 4 + s + (s >= kq)) * 8192 + w * 1024 + l * 16, 0, 16);
#pragma unroll
        for (int i = 0; i < 4; ++i) ok = ok && pr[s][i] != ~0u;
      }
      if (__all(ok)) break;
      asm volatile("" ::: "memory");
    }
#pragma unroll
    for (int i = 0; i < 2; ++i) {
      f32x4 r = *(const f32x4*)(smem + (2 * w + i) * 1024 + l * 16);
#pragma unroll
      for (int s = 0; s < 3; ++s) {
        r[0] += __uint_as_float(pr[s][2 * i] << 16); r[1] += __uint_as_float(pr[s][2 * i] & 0xFFFF0000u);
        r[2] += __uint_as_float(pr[s][2 * i + 1] << 16); r[3] += __uint_as_float(pr[s][2 * i + 1] & 0xFFFF0000u);
      }
      const int mm = mt * 128 + (kq >> 1) * 64 + ((w & 1) * 2 + i) * 16 + c, nn = nt * 128 + (kq & 1) * 64 + (w >> 1) * 16 + g * 4;
      u32x2 pkd = {pk2(r[0], r[1]), pk2(r[2], r[3])};
      *(u32x2*)(out + ((size_t)mm * 2048 + br * 1024 + nn) * 2) = pkd;
    }
  }
  STAMPA(8);
  STAMP_FLUSH(ows);
}

extern "C" void kernel_launch(void* const* d_in, const int* in_sizes, int n_in, void* d_out, int out_size, void* d_ws, size_t ws_size,
                              hipStream_t stream) {
  k1_qproj_attn<<<256, 512, 0, stream>>>((const char*)d_in[0], (const char*)d_in[1], (const char*)d_in[2], (const char*)d_in[3], (const char*)d_in[5],
                                         (const char*)d_in[6], (const char*)d_in[7], (const char*)d_in[8], (char*)d_ws, (const char*)d_in[4],
                                         (const char*)d_in[9], (char*)d_out);
}
```

```cpp
#include <hip/hip_runtime.h>
#include <hip/hip_bf16.h>

#define POL_Q 2
#define POL_KVA 2

typedef short bf16x8 __attribute__((ext_vector_type(8)));
typedef short s16x4 __attribute__((ext_vector_type(4)));
typedef float f32x4 __attribute__((ext_vector_type(4)));
typedef float f32x2 __attribute__((ext_vector_type(2)));
typedef __bf16 bf16x2 __attribute__((ext_vector_type(2)));
typedef unsigned u32x4 __attribute__((ext_vector_type(4)));
typedef unsigned u32x2 __attribute__((ext_vector_type(2)));
#define LAS __attribute__((address_space(3)))
#define DI __device__ __forceinline__

#define WAIT_V(n) asm volatile("s_waitcnt vmcnt(%0)" ::"n"(n) : "memory")
#define WAIT_L0() do { asm volatile("" ::: "memory"); __builtin_amdgcn_s_waitcnt(0xC07F); asm volatile("" ::: "memory"); } while (0)
#define BAR() __builtin_amdgcn_s_barrier()
#define SB() __builtin_amdgcn_sched_barrier(0)

template <bool NT>
DI void glds(const char* sbase, unsigned voff, unsigned ldsaddr) {
  if (NT)
    asm volatile("s_mov_b32 m0, %2\n\ts_nop 0\n\tglobal_load_lds_dwordx4 %0, %1 nt" ::"v"(voff), "s"(sbase), "s"(ldsaddr) : "memory");
  else
    asm volatile("s_mov_b32 m0, %2\n\ts_nop 0\n\tglobal_load_lds_dwordx4 %0, %1" ::"v"(voff), "s"(sbase), "s"(ldsaddr) : "memory");
}
template <bool NT>
DI void glds4(const char* sbase, unsigned v0, unsigned v1, unsigned v2, unsigned v3, unsigned ldsaddr) {
  if (NT)
    asm volatile("s_mov_b32 m0, %5\n\ts_nop 0\n\t"
                 "global_load_lds_dwordx4 %0, %4 nt\n\t"
                 "global_load_lds_dwordx4 %1, %4 offset:1024 nt\n\t"
                 "global_load_lds_dwordx4 %2, %4 offset:2048 nt\n\t"
                 "global_load_lds_dwordx4 %3, %4 offset:3072 nt"
                 ::"v"(v0), "v"(v1), "v"(v2), "v"(v3), "s"(sbase), "s"(ldsaddr) : "memory");
  else
    asm volatile("s_mov_b32 m0, %5\n\ts_nop 0\n\t"
                 "global_load_lds_dwordx4 %0, %4\n\t"
                 "global_load_lds_dwordx4 %1, %4 offset:1024\n\t"
                 "global_load_lds_dwordx4 %2, %4 offset:2048\n\t"
                 "global_load_lds_dwordx4 %3, %4 offset:3072"
                 ::"v"(v0), "v"(v1), "v"(v2), "v"(v3), "s"(sbase), "s"(ldsaddr) : "memory");
}
DI void pin(const char* sbase, unsigned voff, unsigned ldsaddr) {
  asm volatile("s_mov_b32 m0, %2\n\ts_nop 0\n\tglobal_load_lds_dword %0, %1" ::"v"(voff), "s"(sbase), "s"(ldsaddr) : "memory");
}

DI f32x4 mfma16(bf16x8 a, bf16x8 b, f32x4 c) { return __builtin_amdgcn_mfma_f32_16x16x32_bf16(a, b, c, 0, 0, 0); }
DI unsigned pk2(float lo, float hi) { f32x2 a = {lo, hi}; return __builtin_bit_cast(unsigned, __builtin_convertvector(a, bf16x2)); }
DI bf16x8 lds_b128(const char* smem, unsigned off) { return *(const bf16x8*)(smem + off); }
DI s16x4 lds_tr(char* smem, unsigned off) { return __builtin_amdgcn_ds_read_tr16_b64_v4i16((LAS s16x4*)(smem + off)); }
DI bf16x8 cat8(s16x4 lo, s16x4 hi) { return __builtin_shufflevector(lo, hi, 0, 1, 2, 3, 4, 5, 6, 7); }
DI float xg_max(float x) {
  auto s = __builtin_amdgcn_permlane16_swap(__float_as_uint(x), __float_as_uint(x), false, false);
  x = fmaxf(__uint_as_float(s[0]), __uint_as_float(s[1]));
  auto t = __builtin_amdgcn_permlane32_swap(__float_as_uint(x), __float_as_uint(x), false, false);
  return fmaxf(__uint_as_float(t[0]), __uint_as_float(t[1]));
}
DI float xg_sum(float x) {
  auto s = __builtin_amdgcn_permlane16_swap(__float_as_uint(x), __float_as_uint(x), false, false);
  x = __uint_as_float(s[0]) + __uint_as_float(s[1]);
  auto t = __builtin_amdgcn_permlane32_swap(__float_as_uint(x), __float_as_uint(x), false, false);
  return __uint_as_float(t[0]) + __uint_as_float(t[1]);
}

#define STAMP(k)
#define STAMP_DECL
#define STAMP_FLUSH(ws)
constexpr float SL2E = 0.088388347648318447f * 1.4426950408889634f;

constexpr size_t PART_OFF = 16u << 20, CNT_OFF = 8u << 20, Q_OFF = 10u << 20;
#define ASLEEP 0

#define G8_(j, off, sfx) "global_load_lds_dwordx4 %" #j ", %8 offset:" #off sfx "\n\t"
template <int MODE>
DI void glds8(const char* sbase, const unsigned (&v)[8], unsigned ldsaddr) {
  if (MODE == 0)
    asm volatile("s_mov_b32 m0, %9\n\ts_nop 0\n\t" G8_(0, -4096, "") G8_(1, -3072, "") G8_(2, -2048, "") G8_(3, -1024, "") G8_(4, 0, "") G8_(5, 1024, "") G8_(6, 2048, "") G8_(7, 3072, "")
                 ::"v"(v[0]), "v"(v[1]), "v"(v[2]), "v"(v[3]), "v"(v[4]), "v"(v[5]), "v"(v[6]), "v"(v[7]), "s"(sbase), "s"(ldsaddr) : "memory");
  else if (MODE == 3)
    asm volatile("s_mov_b32 m0, %9\n\ts_nop 0\n\t" G8_(0, -4096, " sc1") G8_(1, -3072, " sc1") G8_(2, -2048, " sc1") G8_(3, -1024, " sc1") G8_(4, 0, " sc1") G8_(5, 1024, " sc1") G8_(6, 2048, " sc1") G8_(7, 3072, " sc1")
                 ::"v"(v[0]), "v"(v[1]), "v"(v[2]), "v"(v[3]), "v"(v[4]), "v"(v[5]), "v"(v[6]), "v"(v[7]), "s"(sbase), "s"(ldsaddr) : "memory");
  else if (MODE == 1)
    asm volatile("s_mov_b32 m0, %9\n\ts_nop 0\n\t" G8_(0, -4096, " nt") G8_(1, -3072, " nt") G8_(2, -2048, " nt") G8_(3, -1024, " nt") G8_(4, 0, " nt") G8_(5, 1024, " nt") G8_(6, 2048, " nt") G8_(7, 3072, " nt")
                 ::"v"(v[0]), "v"(v[1]), "v"(v[2]), "v"(v[3]), "v"(v[4]), "v"(v[5]), "v"(v[6]), "v"(v[7]), "s"(sbase), "s"(ldsaddr) : "memory");
  else
    asm volatile("s_mov_b32 m0, %9\n\ts_nop 0\n\t" G8_(0, -4096, " nt") G8_(0, -4096, "") G8_(1, -3072, " nt") G8_(1, -3072, "") G8_(2, -2048, " nt") G8_(2, -2048, "") G8_(3, -1024, " nt") G8_(3, -1024, "")
                 G8_(4, 0, " nt") G8_(4, 0, "") G8_(5, 1024, " nt") G8_(5, 1024, "") G8_(6, 2048, " nt") G8_(6, 2048, "") G8_(7, 3072, " nt") G8_(7, 3072, "")
                 ::"v"(v[0]), "v"(v[1]), "v"(v[2]), "v"(v[3]), "v"(v[4]), "v"(v[5]), "v"(v[6]), "v"(v[7]), "s"(sbase), "s"(ldsaddr) : "memory");
}
#define CVAR 1
#define RPG 2
#define H0SLEEP 12
#define KVAPOL 0
#define BLKBAR 0
#define BLKBAR2 0
#define BNT 1
#define FNT 0
#define WOPF 0
#define NT_VB 1
#define NT_KB 1
#define NT_A 0
#define TLBW 0
#define OAGATE 0
#define PRIO 2
#define TROT 1
#define QPF 1
#define F1LATE 1
#define XPF 0
#define OUTPOL 0
#define KARG 1
#define WPF2 0
#define FDUP 1
#define GSARG
#define GSPASS
#define GSPASS2
#define GBAR() BAR()
#define GST(k)
#define RLX_AGENT __ATOMIC_RELAXED, __HIP_MEMORY_SCOPE_AGENT
DI void poll_ge(unsigned* p, unsigned want) {
  unsigned spins = 0;
  while (__hip_atomic_load(p, RLX_AGENT) < want && ++spins < (1u << 22)) __builtin_amdgcn_s_sleep(1);
  asm volatile("" ::: "memory");
}
DI void tile_ld(u32x4 (&r)[4], const char* sbase, unsigned v0, unsigned v1, unsigned v2, unsigned v3) {
#define TLS ""
  asm volatile("global_load_dwordx4 %0, %4, %8" TLS "\n\tglobal_load_dwordx4 %1, %5, %8 offset:1024" TLS "\n\t"
               "global_load_dwordx4 %2, %6, %8 offset:2048" TLS "\n\tglobal_load_dwordx4 %3, %7, %8 offset:3072" TLS
               : "=&v"(r[0]), "=&v"(r[1]), "=&v"(r[2]), "=&v"(r[3]) : "v"(v0), "v"(v1), "v"(v2), "v"(v3), "s"(sbase) : "memory");
}
DI void glds_wx(const char* wbase, const char* xbase, const unsigned (&v)[8], unsigned ldsaddr) {
#define GW_(j, off) "global_load_lds_dwordx4 %" #j ", %8 offset:" #off "\n\t"
#define GX_(j, off) "global_load_lds_dwordx4 %" #j ", %9 offset:" #off "\n\t"
  asm volatile("s_mov_b32 m0, %10\n\ts_nop 0\n\t" GW_(0, -4096) GW_(1, -3072) GW_(2, -2048) GW_(3, -1024) GX_(4, 0) GX_(5, 1024) GX_(6, 2048) GX_(7, 3072)
               ::"v"(v[0]), "v"(v[1]), "v"(v[2]), "v"(v[3]), "v"(v[4]), "v"(v[5]), "v"(v[6]), "v"(v[7]), "s"(wbase), "s"(xbase), "s"(ldsaddr) : "memory");
}
DI u32x4 pair16(const f32x4& r0, const f32x4& r1) {
  auto x = __builtin_amdgcn_permlane16_swap(pk2(r0[0], r0[1]), pk2(r1[0], r1[1]), false, false);
  auto y = __builtin_amdgcn_permlane16_swap(pk2(r0[2], r0[3]), pk2(r1[2], r1[3]), false, false);
  return u32x4{x[0], y[0], x[1], y[1]};
}
struct HFrags { s16x4 a[4][2]; bf16x8 b[4]; };
template <int WS, int XT, bool TAIL, class TailF, class LandF, class DumpF>
DI void gemm_core(char* smem, unsigned lbase, const char* wg, const char* xg, int rot, int w, int l, f32x4 (&acc)[4][4], TailF tail, LandF land, DumpF dump, bool dotail, unsigned* gate GSARG) {
  int bn_ = XT ? 21 : 1; (void)bn_;
  const int c = l & 15, g = l >> 4, q4 = c >> 2, p = l & 3;
  if (w >= 4) {
    u32x4 lr[10][4];
    if (TAIL && !dotail) {
#pragma unroll
      for (int k = 0; k < 10; ++k) land(k, lr[k]);
      WAIT_V(0);
    }
    const int iw = w & 1;
    unsigned vw[8], vx[8];
    {
      const unsigned vw0 = (unsigned)(g * WS + ((c ^ (2 * g)) * 16)), vw1 = vw0 ^ 128;
      const unsigned vx0 = XT ? (unsigned)(l * 16) : (unsigned)((l >> 3) * 2048 + (((l & 7) ^ g) * 16));
      const unsigned vx1 = XT ? vx0 : (vx0 ^ 64);
#pragma unroll
      for (int j = 0; j < 8; ++j) {
        vw[j] = ((j & 2) ? vw1 : vw0) + j * (4 * WS - 1024);
        vx[j] = XT ? vx0 + (j >> 1) * (128 * 1024 - 2048) : ((j & 1) ? vx1 : vx0) + j * (16384 - 1024);
      }
    }
    const char* w_w = wg + (size_t)(32 * iw) * WS + 4096;
    const char* x_w = (XT ? xg + (size_t)(4 * iw) * (128 * 1024) : xg + (size_t)(64 * iw) * 2048) + 4096;
    const unsigned lw0 = lbase + iw * 8192 + 4096;
    auto wpieces = [&](int kap, unsigned bufoff, bool first) {
      if (first && FNT) glds8<FDUP ? 2 : 1>(w_w + (size_t)kap * (64 * WS), vw, lw0 + bufoff);
      else glds8<0>(w_w + (size_t)kap * (64 * WS), vw, lw0 + bufoff);
    };
    auto xpieces = [&](int kap, unsigned bufoff) { glds8<XT ? 3 : 0>(x_w + kap * (XT ? 2048 : 128), vx, lw0 + bufoff + 16384); };
    constexpr int FN = (FDUP && FNT) ? 24 : 16;
    if (w < 6) {
      if (XT) {
#pragma unroll 1
        for (int f = 0; f < 2; ++f) wpieces(rot + f, f * 32768, true);
        if (w == 4) poll_ge(gate, 8u);
        BAR();
        GST(20);
#pragma unroll 1
        for (int f = 0; f < 2; ++f) xpieces(rot + f, f * 32768);
        WAIT_V(8);
      } else if (F1LATE) {
        wpieces(rot, 0, true); xpieces(rot, 0);
        WAIT_V(0);
      } else {
#pragma unroll 1
        for (int f = 0; f < 2; ++f) { wpieces(rot + f, f * 32768, true); xpieces(rot + f, f * 32768); }
      }
#pragma unroll 1
      for (int i = 0; i < 2; ++i) {
        WAIT_V(FN);
        GBAR();
        if (F1LATE && !XT && i == 0) { wpieces(rot + 1, 32768, true); xpieces(rot + 1, 32768); }
        GBAR();
        wpieces(rot + i + 2, i * 32768, true); xpieces(rot + i + 2, i * 32768);
        GBAR();
        GBAR();
      }
      WAIT_V(FN);
      GBAR();
      GBAR();
      if (TAIL && dotail) tail(0);
      GBAR();
      if (TAIL && dotail) tail(1);
      GBAR();
      if (TAIL && dotail) { tail(2); WAIT_V(12); } else WAIT_V(0);
      GBAR();
      if (TAIL && dotail) tail(3);
      GBAR();
      GBAR();
      GBAR();
    } else {
      auto stage = [&](int kap, int hb) { wpieces(kap & 15, 65536 + hb * 32768, false); xpieces(kap & 15, 65536 + hb * 32768); };
      __builtin_amdgcn_s_sleep(H0SLEEP);
      if (XT) { wpieces((rot + 4) & 15, 65536, false); BAR(); GST(20); xpieces((rot + 4) & 15, 65536); }
      else stage(rot + 4, 0);
      GBAR();
      stage(rot + 8, 1);
      WAIT_V(16);
      GBAR();
      stage(rot + 12, 2);
#pragma unroll 1
      for (int i = 0; i < 3; ++i) {
        WAIT_V(16);
        GBAR();
        stage(rot + 5 + i, 0);
        WAIT_V(16);
        GBAR();
        stage(rot + 9 + i, 1);
        GBAR();
        stage(rot + 13 + i, 2);
        WAIT_V(32);
        GBAR();
      }
      if (TAIL && dotail) { tail(0); tail(1); WAIT_V(24); } else WAIT_V(16);
      GBAR();
      if (TAIL && dotail) { tail(2); tail(3); WAIT_V(16); } else WAIT_V(0);
      GBAR();
    }
    if (TAIL && !dotail) {
#pragma unroll
      for (int k = 0; k < 8; ++k) dump(k, lr[k]);
      BAR();
      dump(8, lr[8]); dump(9, lr[9]);
      asm volatile("s_waitcnt lgkmcnt(0)" ::: "memory");
    }
  } else {
    if (XT) {
      unsigned vp[8];
      if (WPF2) {
#pragma unroll
        for (int j = 0; j < 8; ++j) vp[j] = (unsigned)(g * WS + c * 16 + j * (4 * WS - 1024));
        glds8<0>(wg + (size_t)((rot + 2) * 64 + 32 * w) * WS + 4096, vp, lbase + 131072 + w * 8192 + 4096);
      }
      BAR(); GST(20);
      if (XPF) {
        __builtin_amdgcn_s_sleep(XPF);
#pragma unroll
        for (int j = 0; j < 8; ++j) vp[j] = l * 16 + (j >> 2) * (128 * 1024 - 4096);
        glds8<3>(xg + (size_t)(2 * w) * (128 * 1024) + (rot + 2) * 2048 + 4096, vp, lbase + 131072 + w * 8192 + 4096);
      }
    }
    const int nh = w & 1, mh = w >> 1;
    unsigned aw[4];
    {
      const int F = 2 * q4 | 8 * (g & 1);
#pragma unroll
      for (int t = 0; t < 4; ++t) aw[t] = (unsigned)((8 * g + q4) * 256 + ((((2 * (4 * nh + t)) ^ F) + (p >> 1)) * 16) + (p & 1) * 8);
    }
    const unsigned ax0 = XT ? (unsigned)(16384 + mh * 8192 + l * 16) : (unsigned)(16384 + (64 * mh + c) * 128 + ((g ^ (c >> 1)) * 16));
    const unsigned ax1 = XT ? ax0 + 1024 : (ax0 ^ 64);
    struct FF { HFrags h[2]; };
    auto mm = [&](const FF& fo) {
#pragma unroll
      for (int ks = 0; ks < 2; ++ks)
#pragma unroll
        for (int t = 0; t < 4; ++t)
#pragma unroll
          for (int v = 0; v < 4; ++v) acc[t][v] = mfma16(cat8(fo.h[ks].a[t][0], fo.h[ks].a[t][1]), fo.h[ks].b[v], acc[t][v]);
    };
    auto full = [&](FF& fn, const FF& fo, unsigned bo) {
#pragma unroll
      for (int ks = 0; ks < 2; ++ks)
#pragma unroll
        for (int t = 0; t < 4; ++t) {
          fn.h[ks].b[t] = lds_b128(smem, (ks ? ax1 : ax0) + bo + t * 2048);
          fn.h[ks].a[t][0] = lds_tr(smem, aw[t] + bo + ks * 8192);
          fn.h[ks].a[t][1] = lds_tr(smem, aw[t] + bo + ks * 8192 + 1024);
        }
      mm(fo);
#pragma unroll
      for (int t = 0; t < 24; ++t) {
        __builtin_amdgcn_sched_group_barrier(0x008, 1, 0);
        __builtin_amdgcn_sched_group_barrier(0x100, 1, 0);
      }
      __builtin_amdgcn_sched_group_barrier(0x008, 8, 0);
      SB();
    };
#pragma unroll
    for (int t = 0; t < 4; ++t)
#pragma unroll
      for (int v = 0; v < 4; ++v) acc[t][v] = f32x4{0, 0, 0, 0};
    FF f0, f1;
#pragma unroll
    for (int ks = 0; ks < 2; ++ks)
#pragma unroll
      for (int t = 0; t < 4; ++t) { f1.h[ks].b[t] = bf16x8{0, 0, 0, 0, 0, 0, 0, 0}; f1.h[ks].a[t][0] = s16x4{0, 0, 0, 0}; f1.h[ks].a[t][1] = s16x4{0, 0, 0, 0}; }
#pragma unroll 1
    for (int n = 0; n < 16; n += 2) {
      const unsigned bo0 = (n & 2) ? 98304 : ((n >> 2) & 1) * 32768, bo1 = (n & 2) ? 131072 : 65536;
      WAIT_L0();
      GBAR();
      full(f0, f1, bo0);
      if (XT && (WPF2 + XPF) != 0) WAIT_V(0);
      WAIT_L0();
      GBAR();
      full(f1, f0, bo1);
    }
    WAIT_L0();
    if (TAIL && !dotail) BAR();
    mm(f1);
  }
}

__global__ __launch_bounds__(512, 2) void k1_qproj_attn(const char* __restrict__ xA, const char* __restrict__ kA, const char* __restrict__ vA,
                                                        const char* __restrict__ wqA, const char* __restrict__ xB, const char* __restrict__ kB,
                                                        const char* __restrict__ vB, const char* __restrict__ wqB, char* __restrict__ ows,
                                                        const char* __restrict__ woA, const char* __restrict__ woB, char* __restrict__ out) {
  __shared__ __attribute__((aligned(1024))) char smem[163840];
  asm volatile("" ::"s"(xA), "s"(kA), "s"(vA), "s"(wqA), "s"(xB), "s"(kB), "s"(vB), "s"(wqB), "s"(ows), "s"(woA), "s"(woB), "s"(out));
  const unsigned lbase = (unsigned)(size_t)(LAS char*)smem;
  const int tid = threadIdx.x, l = tid & 63, w = __builtin_amdgcn_readfirstlane(tid >> 6);
  const int xcd = blockIdx.x & 7, j = blockIdx.x >> 3;
  const int br = j & 1, idx = j >> 1;
  const int bg = idx & 3, qhead = 4 * xcd + (idx >> 2), rot = 4 * (((idx >> 2) + bg) & 3);
  const char* xg = (br ? xB : xA) + (size_t)bg * 128 * 2048;
  const char* wq = (br ? wqB : wqA) + qhead * 256;
  const int c = l & 15, g = l >> 4, q4 = c >> 2, p = l & 3;
  const int u = w & 3, dh = w >> 2;
  const int b = br ? 4 * bg + u : (xcd & 1) * 8 + (idx >> 1);
  const int head = br ? qhead : 8 * (xcd >> 1) + 4 * (idx & 1) + u;
  const unsigned ring = (unsigned)((w >= 4 ? w - 4 : w < 2 ? w + 4 : w + 6) * 16384);
  STAMP_DECL;
  STAMP(0);
  if (TLBW && w == 2) {
    const int kq_ = xcd >> 1, uu = l & 3;
    const size_t kvo = ((size_t)(br ? (4 * bg + uu) * 32 + qhead : b * 4 + kq_) * 512) * 256;
    const char* a;
    if (l < 4) a = wq + (size_t)uu * (2u << 20);
    else if (l < 8) a = (br ? kB : kA) + kvo;
    else if (l < 12) a = (br ? vB : vA) + kvo;
    else if (l < 14) a = (br ? woB : woA) + (size_t)(kq_ * 1024 + (l & 1) * 1023) * 2048;
    else if (l < 16) a = ows + (size_t)br * (4u << 20) + (l & 1) * (2u << 20) + (12u << 10);
    else if (l < 18) a = ows + Q_OFF + (l & 1) * (2u << 20);
    else if (l < 19) a = ows + PART_OFF + (size_t)(br * 32 + ((xcd & 1) * 4 + (idx >> 2)) * 4 + (idx & 3)) * (16 * 8192);
    else a = out;
    if (l < TLBW) { unsigned t_ = __builtin_nontemporal_load((const unsigned*)a); asm volatile("" ::"v"(t_)); }
  }
  if (ASLEEP && !br) {
#pragma unroll 1
    for (int i = 0; i < ASLEEP; ++i) __builtin_amdgcn_s_sleep(16);
  }
  const size_t kvoff = ((size_t)(br ? b * 32 + head : b * 4 + (xcd >> 1)) * 512 + dh * 256) * 256;
  const char* kg = (br ? kB : kA) + kvoff;
  const char* vg = (br ? vB : vA) + kvoff;
  unsigned vk[4], vv[2];
#pragma unroll
  for (int i = 0; i < 4; ++i) vk[i] = (unsigned)(g * 256 + ((c ^ g ^ (4 * i)) * 16));
#pragma unroll
  for (int i = 0; i < 2; ++i) vv[i] = (unsigned)(g * 256 + ((c ^ (2 * g) ^ (8 * i)) * 16));
  const int r16 = TROT ? (br ? 2 * w + idx : (idx >> 1) * 2 + xcd) : 0;
  auto slot_off = [](int t) constexpr { return t * 4096; };
  auto tile_to = [&](int blk, int isV, int t, unsigned base) {
    const unsigned la = lbase + base + t * 4096;
    const char* src = (isV ? vg : kg) + ((blk * 4 + t + r16) & 15) * 4096;
    const bool nt = br ? (isV ? NT_VB : NT_KB) : NT_A;
    if (nt) {
      if (isV) glds4<true>(src, vv[0], vv[1], vv[0], vv[1], la);
      else glds4<true>(src, vk[0], vk[1], vk[2], vk[3], la);
    } else {
      if (isV) glds4<false>(src, vv[0], vv[1], vv[0], vv[1], la);
      else glds4<false>(src, vk[0], vk[1], vk[2], vk[3], la);
    }
  };
  auto tile_issue = [&](int blk, int isV, int t) { tile_to(blk, isV, t, ring); };

  unsigned* ctl = (unsigned*)(ows + CNT_OFF);
  const __amdgpu_buffer_rsrc_t rc = __builtin_amdgcn_make_buffer_rsrc(ows + CNT_OFF, 0, 65536 + 16384, 0x00020000);
  unsigned nonce = 0;
  auto echo = [&]() {
    const u32x4 r = __builtin_amdgcn_raw_buffer_load_b128(rc, l * 16, 0, 16);
    __builtin_amdgcn_raw_buffer_store_b128(r, rc, 4096 + l * 16, 0, 16);
  };
  if (w == 0) {
    nonce = __hip_atomic_load(ctl + 1024 + blockIdx.x, RLX_AGENT) + 1u;
    if (l == 0) __hip_atomic_store(ctl + blockIdx.x, nonce, RLX_AGENT);
  } else if (w == 1 && blockIdx.x == 0) {
#pragma unroll
    for (int i = 0; i < 6; ++i) __builtin_amdgcn_raw_buffer_store_b32(0u, rc, 8192 + (i * 64 + l) * 128, 0, 16);
    WAIT_V(0);
    __builtin_amdgcn_s_sleep(24);
    echo();
  }
  if (QPF && !br && w < 4) {
    unsigned vp[8];
#pragma unroll
    for (int i = 0; i < 4; ++i) {
      vp[i] = (unsigned)(g * 8192 + c * 16 + i * (4 * 8192 - 1024));
      vp[4 + i] = (unsigned)((l >> 3) * 2048 + (l & 7) * 16 + i * (16384 - 1024));
    }
    const char* wb = wqB + qhead * 256 + (size_t)(16 * w) * 8192 + 4096;
    const char* xb = xB + (size_t)(bg * 128 + 32 * w) * 2048;
#pragma unroll 1
    for (int k = 1; k < 4; ++k) glds_wx(wb + (size_t)(rot + k) * (64 * 8192), xb + (rot + k) * 128, vp, lbase + 131072 + w * 8192 + 4096);
    WAIT_V(0);
  }
  const int kq = xcd >> 1, mt = idx & 3, nl = idx >> 2, nt = (xcd & 1) * 4 + nl;
  const __amdgpu_buffer_rsrc_t rs = __builtin_amdgcn_make_buffer_rsrc(ows + PART_OFF + (size_t)(br * 32 + nt * 4 + mt) * (16 * 8192), 0, 16 * 8192, 0x00020000);
  if (!br && w >= 1 && w < 4) {
    const int src = (w - 1) + ((w - 1) >= kq);
    const __amdgpu_buffer_rsrc_t r2 = __builtin_amdgcn_make_buffer_rsrc(ows + PART_OFF + (size_t)(nt * 4 + mt) * (16 * 8192), 0, 33 * 16 * 8192, 0x00020000);
#pragma unroll
    for (int i = 0; i < 16; ++i)
      __builtin_amdgcn_raw_buffer_store_b128(u32x4{~0u, ~0u, ~0u, ~0u}, r2, (i >> 3) * (32 * 16 * 8192) + (kq * 4 + src) * 8192 + (i & 7) * 1024 + l * 16, 0, 16);
    WAIT_V(0);
  }
  unsigned* cnt = ctl + 2048 + 32;
  auto confirm = [&]() {
    unsigned spins = 0;
    while (__hip_atomic_load(ctl + 1024 + blockIdx.x, RLX_AGENT) != nonce && ++spins < (1u << 18)) __builtin_amdgcn_s_sleep(2);
    if (l == 0) __hip_atomic_fetch_add(ctl + 2048, 1u, RLX_AGENT);
  };
  f32x4 qa[4][4];
  auto land = [&](int k, u32x4 (&r)[4]) {
    const int ph = k >> 1, hf = k & 1;
    const char* src = ((ph & 1) ? vg : kg) + (hf - 1) * 65536 + (((ph >> 1) * 4 + u + r16) & 15) * 4096;
    if (ph & 1) tile_ld(r, src, vv[0], vv[1], vv[0], vv[1]);
    else tile_ld(r, src, vk[0], vk[1], vk[2], vk[3]);
  };
  auto dump = [&](int k, const u32x4 (&r)[4]) {
    const unsigned d = k * 16384 + u * 4096 + l * 16;
    asm volatile("ds_write_b128 %0, %1\n\tds_write_b128 %0, %2 offset:1024\n\tds_write_b128 %0, %3 offset:2048\n\tds_write_b128 %0, %4 offset:3072"
                 ::"v"(lbase + d), "v"(r[0]), "v"(r[1]), "v"(r[2]), "v"(r[3]) : "memory");
  };
  gemm_core<8192, 0, true>(smem, lbase, wq, xg, rot, w, l, qa, [&](int n) { tile_issue(0, 0, n); }, land, dump, br != 0, nullptr GSPASS);
  bf16x8 qf[2][4];
  if (br) {
    if (w < 4) {
#pragma unroll
      for (int t = 0; t < 4; ++t)
#pragma unroll
        for (int v = 0; v < 4; ++v) {
          const int tau = 4 * (w & 1) + t;
          u32x2 pkd = {pk2(qa[t][v][0], qa[t][v][1]), pk2(qa[t][v][2], qa[t][v][3])};
          const unsigned off = 98304 + ((w >> 1) * 4 + v) * 4096 + (tau >> 1) * 1024 + ((2 * (tau & 1) + (g >> 1)) * 16 + c) * 16 + (g & 1) * 8;
          *(u32x2*)(smem + off) = pkd;
        }
    }
    WAIT_L0();
    BAR();
#pragma unroll
    for (int v = 0; v < 2; ++v)
#pragma unroll
      for (int s = 0; s < 4; ++s) qf[v][s] = lds_b128(smem, 98304 + u * 8192 + v * 4096 + s * 1024 + l * 16);
    if (w == 0) confirm();
    if (w < 4) {
#pragma unroll 1
      for (int t = 0; t < 4; ++t) tile_issue(0, 0, t);
    }
  } else {
    const __amdgpu_buffer_rsrc_t rq = __builtin_amdgcn_make_buffer_rsrc(ows + Q_OFF, 0, 4u << 20, 0x00020000);
    if (w < 4) {
      const int qo = ((bg * 8 + (w >> 1) * 4) * 128 + qhead * 4 + (w & 1) * 2) * 1024 + ((g >> 1) * 16 + c) * 16 + (g & 1) * 512;
#pragma unroll
      for (int t = 0; t < 4; t += 2)
#pragma unroll
        for (int v = 0; v < 4; ++v) __builtin_amdgcn_raw_buffer_store_b128(pair16(qa[t][v], qa[t + 1][v]), rq, qo + v * (128 * 1024) + t * 512, 0, 16);
      if (w == 1 && blockIdx.x == 0) echo();
      if (w == 0) confirm();
      WAIT_V(0);
    }
    BAR();
    if (tid == 0) __hip_atomic_fetch_add(cnt + (288 + xcd * 4 + bg) * 32, 1u, RLX_AGENT);
    if (w == 1 && blockIdx.x == 0) {
      unsigned spins = 0;
      while (__hip_atomic_load(ctl + 2048, RLX_AGENT) < 256u && ++spins < (1u << 17)) { echo(); __builtin_amdgcn_s_sleep(8); }
    }
    if (w == 4) poll_ge(cnt + (288 + (head >> 2) * 4 + (b >> 2)) * 32, 4u);
    BAR();
#pragma unroll
    for (int v = 0; v < 2; ++v)
#pragma unroll
      for (int s = 0; s < 4; ++s)
        qf[v][s] = __builtin_bit_cast(bf16x8, __builtin_amdgcn_raw_buffer_load_b128(rq, ((2 * b + v) * 128 + head * 4 + s) * 1024 + l * 16, 0, 16));
    WAIT_V(0);
  }
  STAMP(1);
#define STAMPA(k) STAMP(k)

  unsigned ak[4], av[8];
#pragma unroll
  for (int s = 0; s < 4; ++s) ak[s] = (unsigned)(c * 256 + ((g ^ c ^ (4 * s)) * 16));
  {
    const int row = 4 * g + q4, F = 2 * (row & 7);
#pragma unroll
    for (int t = 0; t < 8; ++t) av[t] = (unsigned)(row * 256 + ((((2 * t) ^ F) + (p >> 1)) * 16) + (p & 1) * 8);
  }
  f32x4 o[8][2];
#pragma unroll
  for (int t = 0; t < 8; ++t) { o[t][0] = f32x4{0, 0, 0, 0}; o[t][1] = f32x4{0, 0, 0, 0}; }
  unsigned tb = ring, pnext = dh * 16384u, pfree = 4 * 32768u + dh * 16384u;
  float m[2] = {-__builtin_inff(), -__builtin_inff()}, ls[2] = {0.f, 0.f};
#pragma unroll 1
  for (int blk = 0; blk < 4; ++blk) {
    f32x4 sc[4][2];
    if (PRIO && br) {
      if (PRIO == 1) { if (blk == 0) __builtin_amdgcn_s_setprio(3); else if (blk == 1) __builtin_amdgcn_s_setprio(2); else if (blk == 2) __builtin_amdgcn_s_setprio(1); else __builtin_amdgcn_s_setprio(0); }
      else { if (blk == 0) __builtin_amdgcn_s_setprio(3); else if (blk == 2) __builtin_amdgcn_s_setprio(2); else if (blk == 3) __builtin_amdgcn_s_setprio(0); }
    }
    if (!br) {
      if (blk < 3) WAIT_V(12); else WAIT_V(4);
      BAR();
      if (blk == 1) tile_to(blk + 2, 0, u, pfree);
      pfree = tb; tb = pnext; pnext = pnext >= 4 * 32768u ? pnext - 4 * 32768u : pnext + 32768u;
    }
#pragma unroll
    for (int t = 0; t < 4; ++t) {
      if (br) WAIT_V(12);
      bf16x8 kf[4];
#pragma unroll
      for (int s = 0; s < 4; ++s) kf[s] = lds_b128(smem, ak[s] + tb + slot_off(t));
      WAIT_L0();
      if (br) tile_issue(blk, 1, t);
      sc[t][0] = f32x4{0, 0, 0, 0}; sc[t][1] = f32x4{0, 0, 0, 0};
#pragma unroll
      for (int s = 0; s < 4; ++s)
#pragma unroll
        for (int v = 0; v < 2; ++v) sc[t][v] = mfma16(kf[s], qf[v][s], sc[t][v]);
      SB();
    }
    bf16x8 pf[2][2];
#pragma unroll
    for (int v = 0; v < 2; ++v) {
      float mx = sc[0][v][0];
#pragma unroll
      for (int t = 0; t < 4; ++t)
#pragma unroll
        for (int i = 0; i < 4; ++i) mx = fmaxf(mx, sc[t][v][i]);
      mx = xg_max(mx) * SL2E;
      const float mn = fmaxf(m[v], mx);
      const float alpha = __builtin_amdgcn_exp2f(m[v] - mn);
      ls[v] *= alpha;
#pragma unroll
      for (int t = 0; t < 8; ++t) o[t][v] *= alpha;
      m[v] = mn;
      float sum = 0.f;
#pragma unroll
      for (int t = 0; t < 4; ++t)
#pragma unroll
        for (int i = 0; i < 4; ++i) { const float e = __builtin_amdgcn_exp2f(fmaf(sc[t][v][i], SL2E, -mn)); sc[t][v][i] = e; sum += e; }
      ls[v] += sum;
#pragma unroll
      for (int kb = 0; kb < 2; ++kb) {
        u32x4 pw = {pk2(sc[2 * kb][v][0], sc[2 * kb][v][1]), pk2(sc[2 * kb][v][2], sc[2 * kb][v][3]),
                    pk2(sc[2 * kb + 1][v][0], sc[2 * kb + 1][v][1]), pk2(sc[2 * kb + 1][v][2], sc[2 * kb + 1][v][3])};
        pf[kb][v] = __builtin_bit_cast(bf16x8, pw);
      }
    }
    const bool more = blk < 3;
    if (PRIO == 2 && br && blk == 2) __builtin_amdgcn_s_setprio(1);
    if (!br) {
      if (blk < 2) WAIT_V(12); else if (blk == 2) WAIT_V(8); else WAIT_V(0);
      BAR();
      pfree = tb;
      if (blk < 2) tile_to(blk + 2, 1, u, pfree);
      tb = pnext; pnext = pnext >= 4 * 32768u ? pnext - 4 * 32768u : pnext + 32768u;
      pfree = tb;
    }
#pragma unroll
    for (int kb = 0; kb < 2; ++kb) {
      s16x4 vlo[8], vhi[8];
      if (br) { if (kb == 0 || more) WAIT_V(12); else WAIT_V(4); }
#pragma unroll
      for (int t = 0; t < 8; ++t) vlo[t] = lds_tr(smem, av[t] + tb + slot_off(2 * kb));
      if (br) {
        WAIT_L0();
        if (more) { tile_issue(blk + 1, 0, 2 * kb); WAIT_V(12); }
        else if (kb == 0) WAIT_V(8);
        else WAIT_V(0);
      }
#pragma unroll
      for (int t = 0; t < 8; ++t) vhi[t] = lds_tr(smem, av[t] + tb + slot_off(2 * kb + 1));
      WAIT_L0();
      if (br && more) tile_issue(blk + 1, 0, 2 * kb + 1);
#pragma unroll
      for (int t = 0; t < 8; ++t)
#pragma unroll
        for (int v = 0; v < 2; ++v) o[t][v] = mfma16(cat8(vlo[t], vhi[t]), pf[kb][v], o[t][v]);
      SB();
    }
    STAMPA(2 + blk);
  }
  if (!br) BAR();
  ls[0] = xg_sum(ls[0]);
  ls[1] = xg_sum(ls[1]);
  if (dh) {
#pragma unroll
    for (int t = 0; t < 8; ++t) { const f32x4 tmp = o[t][0]; o[t][0] = o[t][1]; o[t][1] = tmp; }
    float tm = m[0]; m[0] = m[1]; m[1] = tm;
    tm = ls[0]; ls[0] = ls[1]; ls[1] = tm;
  }
  {
#pragma unroll
    for (int t = 0; t < 8; ++t) *(f32x4*)(smem + ring + t * 1024 + l * 16) = o[t][1];
    f32x2 ml = {m[1], ls[1]};
    *(f32x2*)(smem + ring + 8192 + l * 8) = ml;
  }
  WAIT_L0();
  BAR();
  {
    const int pw = w ^ 4;
    const unsigned pring = (unsigned)((pw >= 4 ? pw - 4 : pw < 2 ? pw + 4 : pw + 6) * 16384);
    const f32x2 ml = *(const f32x2*)(smem + pring + 8192 + l * 8);
    const float mo = m[0], lo = ls[0];
    const float mm = fmaxf(mo, ml[0]);
    float a0 = __builtin_amdgcn_exp2f(mo - mm), a1 = __builtin_amdgcn_exp2f(ml[0] - mm);
    const float inv = 1.0f / (lo * a0 + ml[1] * a1);
    a0 *= inv; a1 *= inv;
    const __amdgpu_buffer_rsrc_t ro = __builtin_amdgcn_make_buffer_rsrc(ows + (size_t)br * (4u << 20) + ((size_t)(2 * b + dh) * 128 + head * 4) * 1024, 0, 4096, 0x00020000);
#pragma unroll
    for (int t = 0; t < 8; t += 2) {
      const f32x4 r0 = o[t][0] * a0 + *(const f32x4*)(smem + pring + t * 1024 + l * 16) * a1;
      const f32x4 r1 = o[t + 1][0] * a0 + *(const f32x4*)(smem + pring + (t + 1) * 1024 + l * 16) * a1;
      __builtin_amdgcn_raw_buffer_store_b128(pair16(r0, r1), ro, (t + (g & 1)) * 512 + ((g >> 1) * 16 + c) * 16, 0, 16);
    }
  }
  WAIT_V(0);
  WAIT_L0();
  BAR();
  if (tid == 0) __hip_atomic_fetch_add(cnt + ((br * 4 + (b >> 2)) * 4 + (head >> 3)) * 32, 1u, RLX_AGENT);
  STAMPA(6);

  {
    if (w == 5) poll_ge(ctl + 2048, 256u);
    if (WOPF && !br && w < 4) {
      unsigned vp[8];
#pragma unroll
      for (int i = 0; i < 8; ++i) vp[i] = l * 16 + i * 1024;
      const char* src = woB + (size_t)(kq * 1024 + idx * 64 + w * 16) * 2048 + (xcd & 1) * 1024 + 4096;
      glds8<0>(src, vp, lbase + 98304 + w * 16384 + 4096);
      glds8<0>(src + 8 * 2048, vp, lbase + 98304 + w * 16384 + 8192 + 4096);
      WAIT_V(0);
    }
    if (OAGATE && !br) {
      if (w == 4) poll_ge(cnt + ((4 + mt) * 4 + kq) * 32, OAGATE);
      BAR();
    }
    const char* og = ows + (size_t)br * (4u << 20) + ((size_t)(mt * 8) * 128 + kq * 32) * 1024;
    const char* wo = (br ? woB : woA) + (size_t)(kq * 1024) * 2048 + nt * 256;
    f32x4 acc[4][4];
    gemm_core<2048, 1, false>(smem, lbase, wo, og, 4 * ((mt + nl) & 3), w, l, acc, [](int) {}, [](int, u32x4 (&)[4]) {}, [](int, const u32x4 (&)[4]) {}, false, cnt + ((br * 4 + mt) * 4 + kq) * 32 GSPASS2);
    STAMPA(7);
    if (w < 4) {
      if (w != kq) {
        const int off = (w * 4 + kq) * 8192 + l * 16;
#pragma unroll
        for (int t = 0; t < 4; ++t)
#pragma unroll
          for (int h = 0; h < 2; ++h) {
            u32x4 pk = {pk2(acc[t][2 * h][0], acc[t][2 * h][1]), pk2(acc[t][2 * h][2], acc[t][2 * h][3]),
                        pk2(acc[t][2 * h + 1][0], acc[t][2 * h + 1][1]), pk2(acc[t][2 * h + 1][2], acc[t][2 * h + 1][3])};
#pragma unroll
            for (int i = 0; i < 4; ++i) pk[i] = min(pk[i], 0xFFFFFFFEu);
            __builtin_amdgcn_raw_buffer_store_b128(pk, rs, off + (t * 2 + h) * 1024, 0, 16);
          }
      } else {
#pragma unroll
        for (int t = 0; t < 4; ++t)
#pragma unroll
          for (int v = 0; v < 4; ++v) *(f32x4*)(smem + (t * 4 + v) * 1024 + l * 16) = acc[t][v];
        WAIT_L0();
      }
    }
    GST(38);
    BAR();
    u32x4 pr[3];
    for (unsigned spins = 0; spins < (1u << 18); ++spins) {
      bool ok = true;
#pragma unroll
      for (int s = 0; s < 3; ++s) {
        pr[s] = __builtin_amdgcn_raw_buffer_load_b128(rs, (kq * 4 + s + (s >= kq)) * 8192 + w * 1024 + l * 16, 0, 16);
#pragma unroll
        for (int i = 0; i < 4; ++i) ok = ok && pr[s][i] != ~0u;
      }
      if (__all(ok)) break;
      asm volatile("" ::: "memory");
    }
#pragma unroll
    for (int i = 0; i < 2; ++i) {
      f32x4 r = *(const f32x4*)(smem + (2 * w + i) * 1024 + l * 16);
#pragma unroll
      for (int s = 0; s < 3; ++s) {
        r[0] += __uint_as_float(pr[s][2 * i] << 16); r[1] += __uint_as_float(pr[s][2 * i] & 0xFFFF0000u);
        r[2] += __uint_as_float(pr[s][2 * i + 1] << 16); r[3] += __uint_as_float(pr[s][2 * i + 1] & 0xFFFF0000u);
      }
      const int mm = mt * 128 + (kq >> 1) * 64 + ((w & 1) * 2 + i) * 16 + c, nn = nt * 128 + (kq & 1) * 64 + (w >> 1) * 16 + g * 4;
      u32x2 pkd = {pk2(r[0], r[1]), pk2(r[2], r[3])};
      *(u32x2*)(out + ((size_t)mm * 2048 + br * 1024 + nn) * 2) = pkd;
    }
  }
  STAMPA(8);
  STAMP_FLUSH(ows);
}

extern "C" void kernel_launch(void* const* d_in, const int* in_sizes, int n_in, void* d_out, int out_size, void* d_ws, size_t ws_size,
                              hipStream_t stream) {
  k1_qproj_attn<<<256, 512, 0, stream>>>((const char*)d_in[0], (const char*)d_in[1], (const char*)d_in[2], (const char*)d_in[3], (const char*)d_in[5],
                                         (const char*)d_in[6], (const char*)d_in[7], (const char*)d_in[8], (char*)d_ws, (const char*)d_in[4],
                                         (const char*)d_in[9], (char*)d_out);
}
```

```cpp
#include <hip/hip_runtime.h>
#include <hip/hip_bf16.h>

typedef short bf16x8 __attribute__((ext_vector_type(8)));
typedef short s16x4 __attribute__((ext_vector_type(4)));
typedef float f32x4 __attribute__((ext_vector_type(4)));
typedef float f32x2 __attribute__((ext_vector_type(2)));
typedef __bf16 bf16x2 __attribute__((ext_vector_type(2)));
typedef unsigned u32x4 __attribute__((ext_vector_type(4)));
typedef unsigned u32x2 __attribute__((ext_vector_type(2)));
#define LAS __attribute__((address_space(3)))
#define DI __device__ __forceinline__
#define RLX_AGENT __ATOMIC_RELAXED, __HIP_MEMORY_SCOPE_AGENT
#define SC1 16

#define WAIT_V(n) asm volatile("s_waitcnt vmcnt(%0)" ::"n"(n) : "memory")
#define WAIT_L0() do { asm volatile("" ::: "memory"); __builtin_amdgcn_s_waitcnt(0xC07F); asm volatile("" ::: "memory"); } while (0)
#define BAR() __builtin_amdgcn_s_barrier()
#define SB() __builtin_amdgcn_sched_barrier(0)

#define STAMP(k)
#define STAMP_DECL
#define STAMP_FLUSH(ws)

template <bool NT>
DI void glds4(const char* sbase, unsigned v0, unsigned v1, unsigned v2, unsigned v3, unsigned ldsaddr) {
#define G4_(sfx) "s_mov_b32 m0, %5\n\ts_nop 0\n\tglobal_load_lds_dwordx4 %0, %4" sfx "\n\tglobal_load_lds_dwordx4 %1, %4 offset:1024" sfx \
                 "\n\tglobal_load_lds_dwordx4 %2, %4 offset:2048" sfx "\n\tglobal_load_lds_dwordx4 %3, %4 offset:3072" sfx
  if (NT) asm volatile(G4_(" nt")::"v"(v0), "v"(v1), "v"(v2), "v"(v3), "s"(sbase), "s"(ldsaddr) : "memory");
  else asm volatile(G4_("")::"v"(v0), "v"(v1), "v"(v2), "v"(v3), "s"(sbase), "s"(ldsaddr) : "memory");
}
template <bool COH>
DI void glds8(const char* b0, const char* b1, const unsigned (&v)[8], unsigned ldsaddr) {
#define G8_(j, b, off, sfx) "global_load_lds_dwordx4 %" #j ", %" #b " offset:" #off sfx "\n\t"
#define G8A_(sfx) "s_mov_b32 m0, %10\n\ts_nop 0\n\t" G8_(0, 8, -4096, sfx) G8_(1, 8, -3072, sfx) G8_(2, 8, -2048, sfx) G8_(3, 8, -1024, sfx) \
                  G8_(4, 9, 0, sfx) G8_(5, 9, 1024, sfx) G8_(6, 9, 2048, sfx) G8_(7, 9, 3072, sfx)
  if (COH) asm volatile(G8A_(" sc1")::"v"(v[0]), "v"(v[1]), "v"(v[2]), "v"(v[3]), "v"(v[4]), "v"(v[5]), "v"(v[6]), "v"(v[7]), "s"(b0), "s"(b1), "s"(ldsaddr) : "memory");
  else asm volatile(G8A_("")::"v"(v[0]), "v"(v[1]), "v"(v[2]), "v"(v[3]), "v"(v[4]), "v"(v[5]), "v"(v[6]), "v"(v[7]), "s"(b0), "s"(b1), "s"(ldsaddr) : "memory");
}
DI void tile_ld(u32x4 (&r)[4], const char* sbase, unsigned v0, unsigned v1, unsigned v2, unsigned v3) {
  asm volatile("global_load_dwordx4 %0, %4, %8\n\tglobal_load_dwordx4 %1, %5, %8 offset:1024\n\t"
               "global_load_dwordx4 %2, %6, %8 offset:2048\n\tglobal_load_dwordx4 %3, %7, %8 offset:3072"
               : "=&v"(r[0]), "=&v"(r[1]), "=&v"(r[2]), "=&v"(r[3]) : "v"(v0), "v"(v1), "v"(v2), "v"(v3), "s"(sbase) : "memory");
}
DI void tile_st(unsigned ldsaddr, const u32x4 (&r)[4]) {
  asm volatile("ds_write_b128 %0, %1\n\tds_write_b128 %0, %2 offset:1024\n\tds_write_b128 %0, %3 offset:2048\n\tds_write_b128 %0, %4 offset:3072"
               ::"v"(ldsaddr), "v"(r[0]), "v"(r[1]), "v"(r[2]), "v"(r[3]) : "memory");
}

DI f32x4 mfma16(bf16x8 a, bf16x8 b, f32x4 c) { return __builtin_amdgcn_mfma_f32_16x16x32_bf16(a, b, c, 0, 0, 0); }
DI unsigned pk2(float lo, float hi) { f32x2 a = {lo, hi}; return __builtin_bit_cast(unsigned, __builtin_convertvector(a, bf16x2)); }
DI bf16x8 lds_b128(const char* smem, unsigned off) { return *(const bf16x8*)(smem + off); }
DI s16x4 lds_tr(char* smem, unsigned off) { return __builtin_amdgcn_ds_read_tr16_b64_v4i16((LAS s16x4*)(smem + off)); }
DI bf16x8 cat8(s16x4 lo, s16x4 hi) { return __builtin_shufflevector(lo, hi, 0, 1, 2, 3, 4, 5, 6, 7); }
DI float xg_max(float x) {
  auto s = __builtin_amdgcn_permlane16_swap(__float_as_uint(x), __float_as_uint(x), false, false);
  x = fmaxf(__uint_as_float(s[0]), __uint_as_float(s[1]));
  auto t = __builtin_amdgcn_permlane32_swap(__float_as_uint(x), __float_as_uint(x), false, false);
  return fmaxf(__uint_as_float(t[0]), __uint_as_float(t[1]));
}
DI float xg_sum(float x) {
  auto s = __builtin_amdgcn_permlane16_swap(__float_as_uint(x), __float_as_uint(x), false, false);
  x = __uint_as_float(s[0]) + __uint_as_float(s[1]);
  auto t = __builtin_amdgcn_permlane32_swap(__float_as_uint(x), __float_as_uint(x), false, false);
  return __uint_as_float(t[0]) + __uint_as_float(t[1]);
}
DI u32x4 pair16(const f32x4& r0, const f32x4& r1) {
  auto x = __builtin_amdgcn_permlane16_swap(pk2(r0[0], r0[1]), pk2(r1[0], r1[1]), false, false);
  auto y = __builtin_amdgcn_permlane16_swap(pk2(r0[2], r0[3]), pk2(r1[2], r1[3]), false, false);
  return u32x4{x[0], y[0], x[1], y[1]};
}
DI void poll_ge(unsigned* p, unsigned want) {
  unsigned spins = 0;
  while (__hip_atomic_load(p, RLX_AGENT) < want && ++spins < (1u << 18)) __builtin_amdgcn_s_sleep(1);
  asm volatile("" ::: "memory");
}

constexpr float SL2E = 0.088388347648318447f * 1.4426950408889634f;
constexpr size_t CNT_OFF = 8u << 20, Q_OFF = 10u << 20, PART_OFF = 16u << 20;

struct HFrags { s16x4 a[4][2]; bf16x8 b[4]; };
struct FFrags { HFrags h[2]; };
template <int WS, int XT, class TailF, class LandF, class DumpF>
DI void gemm_core(char* smem, unsigned lbase, const char* wg, const char* xg, int rot, int w, int l, f32x4 (&acc)[4][4], TailF tail, LandF land,
                  DumpF dump, bool stream, unsigned* gate) {
  constexpr bool QP = !XT;
  const int c = l & 15, g = l >> 4, q4 = c >> 2, p = l & 3;
  if (w >= 4) {
    u32x4 lr[10][4];
    if (QP && !stream) {
#pragma unroll
      for (int k = 0; k < 10; ++k) land(k, lr[k]);
      WAIT_V(0);
    }
    const int iw = w & 1;
    unsigned vw[8], vx[8];
    {
      const unsigned vw0 = (unsigned)(g * WS + ((c ^ (2 * g)) * 16)), vw1 = vw0 ^ 128;
      const unsigned vx0 = XT ? (unsigned)(l * 16) : (unsigned)((l >> 3) * 2048 + (((l & 7) ^ g) * 16));
      const unsigned vx1 = vx0 ^ 64;
#pragma unroll
      for (int j = 0; j < 8; ++j) {
        vw[j] = ((j & 2) ? vw1 : vw0) + j * (4 * WS - 1024);
        vx[j] = XT ? vx0 + (j >> 1) * (128 * 1024 - 2048) : ((j & 1) ? vx1 : vx0) + j * (16384 - 1024);
      }
    }
    const char* w_w = wg + (size_t)(32 * iw) * WS + 4096;
    const char* x_w = (XT ? xg + (size_t)(4 * iw) * (128 * 1024) : xg + (size_t)(64 * iw) * 2048) + 4096;
    const unsigned lw0 = lbase + iw * 8192 + 4096;
    auto wpart = [&](int kap, unsigned buf) { const char* s = w_w + (size_t)kap * (64 * WS); glds8<false>(s, s, vw, lw0 + buf); };
    auto xpart = [&](int kap, unsigned buf) { const char* s = x_w + kap * (XT ? 2048 : 128); glds8<XT != 0>(s, s, vx, lw0 + buf + 16384); };
    auto stage = [&](int kap, unsigned buf) { wpart(kap & 15, buf); xpart(kap & 15, buf); };
    if (w < 6) {
      if (XT) {
#pragma unroll 1
        for (int f = 0; f < 2; ++f) wpart(rot + f, f * 32768);
        if (w == 4) poll_ge(gate, 8u);
        BAR();
#pragma unroll 1
        for (int f = 0; f < 2; ++f) xpart(rot + f, f * 32768);
        WAIT_V(8);
      } else {
        stage(rot, 0);
        WAIT_V(0);
      }
#pragma unroll 1
      for (int i = 0; i < 2; ++i) {
        WAIT_V(16);
        BAR();
        if (QP && i == 0) stage(rot + 1, 32768);
        BAR();
        stage(rot + i + 2, i * 32768);
        BAR();
        BAR();
      }
      WAIT_V(16);
      BAR();
      BAR();
      if (QP && stream) tail(0);
      BAR();
      if (QP && stream) tail(1);
      BAR();
      if (QP && stream) { tail(2); WAIT_V(12); } else WAIT_V(0);
      BAR();
      if (QP && stream) tail(3);
      BAR();
      BAR();
      BAR();
    } else {
      __builtin_amdgcn_s_sleep(12);
      wpart((rot + 4) & 15, 65536);
      if (XT) BAR();
      xpart((rot + 4) & 15, 65536);
      BAR();
      stage(rot + 8, 98304);
      WAIT_V(16);
      BAR();
      stage(rot + 12, 131072);
#pragma unroll 1
      for (int i = 0; i < 3; ++i) {
        WAIT_V(16);
        BAR();
        stage(rot + 5 + i, 65536);
        WAIT_V(16);
        BAR();
        stage(rot + 9 + i, 98304);
        BAR();
        stage(rot + 13 + i, 131072);
        WAIT_V(32);
        BAR();
      }
      if (QP && stream) { tail(0); tail(1); WAIT_V(24); } else WAIT_V(16);
      BAR();
      if (QP && stream) { tail(2); tail(3); WAIT_V(16); } else WAIT_V(0);
      BAR();
    }
    if (QP && !stream) {
#pragma unroll
      for (int k = 0; k < 8; ++k) dump(k, lr[k]);
      BAR();
      dump(8, lr[8]);
      dump(9, lr[9]);
      asm volatile("s_waitcnt lgkmcnt(0)" ::: "memory");
    }
  } else {
    if (XT) BAR();
    const int nh = w & 1, mh = w >> 1;
    unsigned aw[4];
    {
      const int F = 2 * q4 | 8 * (g & 1);
#pragma unroll
      for (int t = 0; t < 4; ++t) aw[t] = (unsigned)((8 * g + q4) * 256 + ((((2 * (4 * nh + t)) ^ F) + (p >> 1)) * 16) + (p & 1) * 8);
    }
    const unsigned ax0 = XT ? (unsigned)(16384 + mh * 8192 + l * 16) : (unsigned)(16384 + (64 * mh + c) * 128 + ((g ^ (c >> 1)) * 16));
    const unsigned ax1 = XT ? ax0 + 1024 : (ax0 ^ 64);
    auto mm = [&](const FFrags& fo) {
#pragma unroll
      for (int ks = 0; ks < 2; ++ks)
#pragma unroll
        for (int t = 0; t < 4; ++t)
#pragma unroll
          for (int v = 0; v < 4; ++v) acc[t][v] = mfma16(cat8(fo.h[ks].a[t][0], fo.h[ks].a[t][1]), fo.h[ks].b[v], acc[t][v]);
    };
    auto step = [&](FFrags& fn, const FFrags& fo, unsigned bo) {
#pragma unroll
      for (int ks = 0; ks < 2; ++ks)
#pragma unroll
        for (int t = 0; t < 4; ++t) {
          fn.h[ks].b[t] = lds_b128(smem, (ks ? ax1 : ax0) + bo + t * 2048);
          fn.h[ks].a[t][0] = lds_tr(smem, aw[t] + bo + ks * 8192);
          fn.h[ks].a[t][1] = lds_tr(smem, aw[t] + bo + ks * 8192 + 1024);
        }
      mm(fo);
#pragma unroll
      for (int t = 0; t < 24; ++t) {
        __builtin_amdgcn_sched_group_barrier(0x008, 1, 0);
        __builtin_amdgcn_sched_group_barrier(0x100, 1, 0);
      }
      __builtin_amdgcn_sched_group_barrier(0x008, 8, 0);
      SB();
    };
#pragma unroll
    for (int t = 0; t < 4; ++t)
#pragma unroll
      for (int v = 0; v < 4; ++v) acc[t][v] = f32x4{0, 0, 0, 0};
    FFrags f0, f1;
#pragma unroll
    for (int ks = 0; ks < 2; ++ks)
#pragma unroll
      for (int t = 0; t < 4; ++t) { f1.h[ks].b[t] = bf16x8{0, 0, 0, 0, 0, 0, 0, 0}; f1.h[ks].a[t][0] = s16x4{0, 0, 0, 0}; f1.h[ks].a[t][1] = s16x4{0, 0, 0, 0}; }
#pragma unroll 1
    for (int n = 0; n < 16; n += 2) {
      const unsigned bo0 = (n & 2) ? 98304 : ((n >> 2) & 1) * 32768, bo1 = (n & 2) ? 131072 : 65536;
      WAIT_L0();
      BAR();
      step(f0, f1, bo0);
      WAIT_L0();
      BAR();
      step(f1, f0, bo1);
    }
    WAIT_L0();
    if (QP && !stream) BAR();
    mm(f1);
  }
}

__global__ __launch_bounds__(512, 2) void fused_blocks(const char* __restrict__ xA, const char* __restrict__ kA, const char* __restrict__ vA,
                                                       const char* __restrict__ wqA, const char* __restrict__ xB, const char* __restrict__ kB,
                                                       const char* __restrict__ vB, const char* __restrict__ wqB, char* __restrict__ ows,
                                                       const char* __restrict__ woA, const char* __restrict__ woB, char* __restrict__ out) {
  __shared__ __attribute__((aligned(1024))) char smem[163840];
  asm volatile("" ::"s"(xA), "s"(kA), "s"(vA), "s"(wqA), "s"(xB), "s"(kB), "s"(vB), "s"(wqB), "s"(ows), "s"(woA), "s"(woB), "s"(out));
  const unsigned lbase = (unsigned)(size_t)(LAS char*)smem;
  const int tid = threadIdx.x, l = tid & 63, w = __builtin_amdgcn_readfirstlane(tid >> 6);
  const int xcd = blockIdx.x & 7, j = blockIdx.x >> 3;
  const int br = j & 1, idx = j >> 1;
  const int bg = idx & 3, qhead = 4 * xcd + (idx >> 2), rot = 4 * (((idx >> 2) + bg) & 3);
  const char* xg = (br ? xB : xA) + (size_t)bg * 128 * 2048;
  const char* wq = (br ? wqB : wqA) + qhead * 256;
  const int c = l & 15, g = l >> 4, q4 = c >> 2, p = l & 3;
  const int u = w & 3, dh = w >> 2;
  const int b = br ? 4 * bg + u : (xcd & 1) * 8 + (idx >> 1);
  const int head = br ? qhead : 8 * (xcd >> 1) + 4 * (idx & 1) + u;
  auto ring_of = [](int ww) { return (unsigned)((ww >= 4 ? ww - 4 : ww < 2 ? ww + 4 : ww + 6) * 16384); };
  const unsigned ring = ring_of(w);
  STAMP_DECL;
  STAMP(0);
  if (w == 2 && l < 12) {
    const int uu = l & 3;
    const size_t kvo = ((size_t)(br ? (4 * bg + uu) * 32 + qhead : b * 4 + (xcd >> 1)) * 512) * 256;
    const char* a = l < 4 ? wq + (size_t)uu * (2u << 20) : l < 8 ? (br ? kB : kA) + kvo : (br ? vB : vA) + kvo;
    const unsigned t_ = __builtin_nontemporal_load((const unsigned*)a);
    asm volatile("" ::"v"(t_));
  }
  const size_t kvoff = ((size_t)(br ? b * 32 + head : b * 4 + (xcd >> 1)) * 512 + dh * 256) * 256;
  const char* kg = (br ? kB : kA) + kvoff;
  const char* vg = (br ? vB : vA) + kvoff;
  unsigned vk[4], vv[2];
#pragma unroll
  for (int i = 0; i < 4; ++i) vk[i] = (unsigned)(g * 256 + ((c ^ g ^ (4 * i)) * 16));
#pragma unroll
  for (int i = 0; i < 2; ++i) vv[i] = (unsigned)(g * 256 + ((c ^ (2 * g) ^ (8 * i)) * 16));
  const int r16 = br ? 2 * w + idx : (idx >> 1) * 2 + xcd;
  auto tile_to = [&](int blk, int isV, int t, unsigned base) {
    const unsigned la = lbase + base + t * 4096;
    const char* src = (isV ? vg : kg) + ((blk * 4 + t + r16) & 15) * 4096;
    if (br) {
      if (isV) glds4<true>(src, vv[0], vv[1], vv[0], vv[1], la);
      else glds4<true>(src, vk[0], vk[1], vk[2], vk[3], la);
    } else {
      if (isV) glds4<false>(src, vv[0], vv[1], vv[0], vv[1], la);
      else glds4<false>(src, vk[0], vk[1], vk[2], vk[3], la);
    }
  };
  auto tile_issue = [&](int blk, int isV, int t) { tile_to(blk, isV, t, ring); };

  unsigned* ctl = (unsigned*)(ows + CNT_OFF);
  const __amdgpu_buffer_rsrc_t rc = __builtin_amdgcn_make_buffer_rsrc(ows + CNT_OFF, 0, 32768, 0x00020000);
  unsigned nonce = 0;
  auto echo = [&]() {
    const u32x4 r = __builtin_amdgcn_raw_buffer_load_b128(rc, l * 16, 0, SC1);
    __builtin_amdgcn_raw_buffer_store_b128(r, rc, 4096 + l * 16, 0, SC1);
  };
  if (w == 0) {
    nonce = __hip_atomic_load(ctl + 1024 + blockIdx.x, RLX_AGENT) + 1u;
    if (l == 0) __hip_atomic_store(ctl + blockIdx.x, nonce, RLX_AGENT);
  } else if (w == 1 && blockIdx.x == 0) {
#pragma unroll
    for (int i = 0; i < 2; ++i) __builtin_amdgcn_raw_buffer_store_b32(0u, rc, 8192 + (i * 64 + l) * 128, 0, SC1);
    WAIT_V(0);
    __builtin_amdgcn_s_sleep(24);
    echo();
  }
  unsigned* cnt = ctl + 2048 + 32;
  auto confirm = [&]() {
    unsigned spins = 0;
    while (__hip_atomic_load(ctl + 1024 + blockIdx.x, RLX_AGENT) != nonce && ++spins < (1u << 18)) __builtin_amdgcn_s_sleep(2);
    if (l == 0) __hip_atomic_fetch_add(ctl + 2048, 1u, RLX_AGENT);
  };
  const int kq = xcd >> 1, mt = idx & 3, nl = idx >> 2, nt = (xcd & 1) * 4 + nl;
  if (!br && w < 4) {
    unsigned vp[8];
#pragma unroll
    for (int i = 0; i < 4; ++i) {
      vp[i] = (unsigned)(g * 8192 + c * 16 + i * (4 * 8192 - 1024));
      vp[4 + i] = (unsigned)((l >> 3) * 2048 + (l & 7) * 16 + i * (16384 - 1024));
    }
    const char* wb = wqB + qhead * 256 + (size_t)(16 * w) * 8192 + 4096;
    const char* xb = xB + (size_t)(bg * 128 + 32 * w) * 2048;
#pragma unroll 1
    for (int k = 1; k < 4; ++k) glds8<false>(wb + (size_t)(rot + k) * (64 * 8192), xb + (rot + k) * 128, vp, lbase + 131072 + w * 8192 + 4096);
    WAIT_V(0);
    if (w >= 1) {
      const int src = (w - 1) + ((w - 1) >= kq);
      const __amdgpu_buffer_rsrc_t r2 = __builtin_amdgcn_make_buffer_rsrc(ows + PART_OFF + (size_t)(nt * 4 + mt) * (16 * 8192), 0, 33 * 16 * 8192, 0x00020000);
#pragma unroll
      for (int i = 0; i < 16; ++i)
        __builtin_amdgcn_raw_buffer_store_b128(u32x4{~0u, ~0u, ~0u, ~0u}, r2, (i >> 3) * (32 * 16 * 8192) + (kq * 4 + src) * 8192 + (i & 7) * 1024 + l * 16, 0, SC1);
      WAIT_V(0);
    }
  }

  f32x4 qa[4][4];
  auto land = [&](int k, u32x4 (&r)[4]) {
    const int ph = k >> 1, hf = k & 1;
    const char* src = ((ph & 1) ? vg : kg) + (hf - 1) * 65536 + (((ph >> 1) * 4 + u + r16) & 15) * 4096;
    if (ph & 1) tile_ld(r, src, vv[0], vv[1], vv[0], vv[1]);
    else tile_ld(r, src, vk[0], vk[1], vk[2], vk[3]);
  };
  auto dump = [&](int k, const u32x4 (&r)[4]) { tile_st(lbase + k * 16384 + u * 4096 + l * 16, r); };
  gemm_core<8192, 0>(smem, lbase, wq, xg, rot, w, l, qa, [&](int n) { tile_issue(0, 0, n); }, land, dump, br != 0, nullptr);
  bf16x8 qf[2][4];
  if (br) {
    if (w < 4) {
#pragma unroll
      for (int t = 0; t < 4; ++t)
#pragma unroll
        for (int v = 0; v < 4; ++v) {
          const unsigned off = 98304 + ((w >> 1) * 4 + v) * 4096 + (w & 1) * 2048 + t * 512 + ((g >> 1) * 16 + c) * 16 + (g & 1) * 8;
          *(u32x2*)(smem + off) = u32x2{pk2(qa[t][v][0], qa[t][v][1]), pk2(qa[t][v][2], qa[t][v][3])};
        }
    }
    WAIT_L0();
    BAR();
#pragma unroll
    for (int v = 0; v < 2; ++v)
#pragma unroll
      for (int s = 0; s < 4; ++s) qf[v][s] = lds_b128(smem, 98304 + u * 8192 + v * 4096 + s * 1024 + l * 16);
    if (w == 0) confirm();
    if (w < 4) {
#pragma unroll 1
      for (int t = 0; t < 4; ++t) tile_issue(0, 0, t);
    }
  } else {
    const __amdgpu_buffer_rsrc_t rq = __builtin_amdgcn_make_buffer_rsrc(ows + Q_OFF, 0, 4u << 20, 0x00020000);
    if (w < 4) {
      const int qo = ((bg * 8 + (w >> 1) * 4) * 128 + qhead * 4 + (w & 1) * 2) * 1024 + ((g >> 1) * 16 + c) * 16 + (g & 1) * 512;
#pragma unroll
      for (int t = 0; t < 4; t += 2)
#pragma unroll
        for (int v = 0; v < 4; ++v) __builtin_amdgcn_raw_buffer_store_b128(pair16(qa[t][v], qa[t + 1][v]), rq, qo + v * (128 * 1024) + t * 512, 0, SC1);
      if (w == 1 && blockIdx.x == 0) echo();
      if (w == 0) confirm();
      WAIT_V(0);
    }
    BAR();
    if (tid == 0) __hip_atomic_fetch_add(cnt + (32 + xcd * 4 + bg) * 32, 1u, RLX_AGENT);
    if (w == 1 && blockIdx.x == 0) {
      unsigned spins = 0;
      while (__hip_atomic_load(ctl + 2048, RLX_AGENT) < 256u && ++spins < (1u << 17)) { echo(); __builtin_amdgcn_s_sleep(8); }
    }
    if (w == 4) poll_ge(cnt + (32 + (head >> 2) * 4 + (b >> 2)) * 32, 4u);
    BAR();
#pragma unroll
    for (int v = 0; v < 2; ++v)
#pragma unroll
      for (int s = 0; s < 4; ++s)
        qf[v][s] = __builtin_bit_cast(bf16x8, __builtin_amdgcn_raw_buffer_load_b128(rq, ((2 * b + v) * 128 + head * 4 + s) * 1024 + l * 16, 0, SC1));
    WAIT_V(0);
  }
  STAMP(1);

  unsigned ak[4], av[8];
#pragma unroll
  for (int s = 0; s < 4; ++s) ak[s] = (unsigned)(c * 256 + ((g ^ c ^ (4 * s)) * 16));
  {
    const int row = 4 * g + q4, F = 2 * (row & 7);
#pragma unroll
    for (int t = 0; t < 8; ++t) av[t] = (unsigned)(row * 256 + ((((2 * t) ^ F) + (p >> 1)) * 16) + (p & 1) * 8);
  }
  f32x4 o[8][2];
#pragma unroll
  for (int t = 0; t < 8; ++t) { o[t][0] = f32x4{0, 0, 0, 0}; o[t][1] = f32x4{0, 0, 0, 0}; }
  unsigned tb = ring, pnext = dh * 16384u;
  float m[2] = {-__builtin_inff(), -__builtin_inff()}, ls[2] = {0.f, 0.f};
#pragma unroll 1
  for (int blk = 0; blk < 4; ++blk) {
    f32x4 sc[4][2];
    if (br) {
      if (blk == 0) __builtin_amdgcn_s_setprio(3); else if (blk == 2) __builtin_amdgcn_s_setprio(2); else if (blk == 3) __builtin_amdgcn_s_setprio(0);
    } else {
      if (blk == 3) WAIT_V(4);
      BAR();
      if (blk == 1) tile_to(3, 0, u, tb);
      tb = pnext; pnext = pnext >= 4 * 32768u ? pnext - 4 * 32768u : pnext + 32768u;
    }
#pragma unroll
    for (int t = 0; t < 4; ++t) {
      if (br) WAIT_V(12);
      bf16x8 kf[4];
#pragma unroll
      for (int s = 0; s < 4; ++s) kf[s] = lds_b128(smem, ak[s] + tb + t * 4096);
      WAIT_L0();
      if (br) tile_issue(blk, 1, t);
      sc[t][0] = f32x4{0, 0, 0, 0}; sc[t][1] = f32x4{0, 0, 0, 0};
#pragma unroll
      for (int s = 0; s < 4; ++s)
#pragma unroll
        for (int v = 0; v < 2; ++v) sc[t][v] = mfma16(kf[s], qf[v][s], sc[t][v]);
      SB();
    }
    bf16x8 pf[2][2];
#pragma unroll
    for (int v = 0; v < 2; ++v) {
      float mx = sc[0][v][0];
#pragma unroll
      for (int t = 0; t < 4; ++t)
#pragma unroll
        for (int i = 0; i < 4; ++i) mx = fmaxf(mx, sc[t][v][i]);
      mx = xg_max(mx) * SL2E;
      const float mn = fmaxf(m[v], mx);
      const float alpha = __builtin_amdgcn_exp2f(m[v] - mn);
      ls[v] *= alpha;
#pragma unroll
      for (int t = 0; t < 8; ++t) o[t][v] *= alpha;
      m[v] = mn;
      float sum = 0.f;
#pragma unroll
      for (int t = 0; t < 4; ++t)
#pragma unroll
        for (int i = 0; i < 4; ++i) { const float e = __builtin_amdgcn_exp2f(fmaf(sc[t][v][i], SL2E, -mn)); sc[t][v][i] = e; sum += e; }
      ls[v] += sum;
#pragma unroll
      for (int kb = 0; kb < 2; ++kb) {
        u32x4 pw = {pk2(sc[2 * kb][v][0], sc[2 * kb][v][1]), pk2(sc[2 * kb][v][2], sc[2 * kb][v][3]),
                    pk2(sc[2 * kb + 1][v][0], sc[2 * kb + 1][v][1]), pk2(sc[2 * kb + 1][v][2], sc[2 * kb + 1][v][3])};
        pf[kb][v] = __builtin_bit_cast(bf16x8, pw);
      }
    }
    const bool more = blk < 3;
    if (br) {
      if (blk == 2) __builtin_amdgcn_s_setprio(1);
    } else {
      if (blk == 2) WAIT_V(8); else if (blk == 3) WAIT_V(0);
      BAR();
      if (blk < 2) tile_to(blk + 2, 1, u, tb);
      tb = pnext; pnext = pnext >= 4 * 32768u ? pnext - 4 * 32768u : pnext + 32768u;
    }
#pragma unroll
    for (int kb = 0; kb < 2; ++kb) {
      s16x4 vlo[8], vhi[8];
      if (br) { if (kb == 0 || more) WAIT_V(12); else WAIT_V(4); }
#pragma unroll
      for (int t = 0; t < 8; ++t) vlo[t] = lds_tr(smem, av[t] + tb + (2 * kb) * 4096);
      if (br) {
        WAIT_L0();
        if (more) { tile_issue(blk + 1, 0, 2 * kb); WAIT_V(12); }
        else if (kb == 0) WAIT_V(8);
        else WAIT_V(0);
      }
#pragma unroll
      for (int t = 0; t < 8; ++t) vhi[t] = lds_tr(smem, av[t] + tb + (2 * kb + 1) * 4096);
      WAIT_L0();
      if (br && more) tile_issue(blk + 1, 0, 2 * kb + 1);
#pragma unroll
      for (int t = 0; t < 8; ++t)
#pragma unroll
        for (int v = 0; v < 2; ++v) o[t][v] = mfma16(cat8(vlo[t], vhi[t]), pf[kb][v], o[t][v]);
      SB();
    }
    STAMP(2 + blk);
  }
  if (!br) BAR();
  ls[0] = xg_sum(ls[0]);
  ls[1] = xg_sum(ls[1]);
  if (dh) {
#pragma unroll
    for (int t = 0; t < 8; ++t) { const f32x4 tmp = o[t][0]; o[t][0] = o[t][1]; o[t][1] = tmp; }
    float tm = m[0]; m[0] = m[1]; m[1] = tm;
    tm = ls[0]; ls[0] = ls[1]; ls[1] = tm;
  }
#pragma unroll
  for (int t = 0; t < 8; ++t) *(f32x4*)(smem + ring + t * 1024 + l * 16) = o[t][1];
  *(f32x2*)(smem + ring + 8192 + l * 8) = f32x2{m[1], ls[1]};
  WAIT_L0();
  BAR();
  {
    const unsigned pring = ring_of(w ^ 4);
    const f32x2 ml = *(const f32x2*)(smem + pring + 8192 + l * 8);
    const float mm = fmaxf(m[0], ml[0]);
    float a0 = __builtin_amdgcn_exp2f(m[0] - mm), a1 = __builtin_amdgcn_exp2f(ml[0] - mm);
    const float inv = 1.0f / (ls[0] * a0 + ml[1] * a1);
    a0 *= inv; a1 *= inv;
    const __amdgpu_buffer_rsrc_t ro = __builtin_amdgcn_make_buffer_rsrc(ows + (size_t)br * (4u << 20) + ((size_t)(2 * b + dh) * 128 + head * 4) * 1024, 0, 4096, 0x00020000);
#pragma unroll
    for (int t = 0; t < 8; t += 2) {
      const f32x4 r0 = o[t][0] * a0 + *(const f32x4*)(smem + pring + t * 1024 + l * 16) * a1;
      const f32x4 r1 = o[t + 1][0] * a0 + *(const f32x4*)(smem + pring + (t + 1) * 1024 + l * 16) * a1;
      __builtin_amdgcn_raw_buffer_store_b128(pair16(r0, r1), ro, (t + (g & 1)) * 512 + ((g >> 1) * 16 + c) * 16, 0, SC1);
    }
  }
  WAIT_V(0);
  WAIT_L0();
  BAR();
  if (tid == 0) __hip_atomic_fetch_add(cnt + ((br * 4 + (b >> 2)) * 4 + (head >> 3)) * 32, 1u, RLX_AGENT);
  STAMP(6);

  if (w == 5) poll_ge(ctl + 2048, 256u);
  const char* og = ows + (size_t)br * (4u << 20) + ((size_t)(mt * 8) * 128 + kq * 32) * 1024;
  const char* wo = (br ? woB : woA) + (size_t)(kq * 1024) * 2048 + nt * 256;
  f32x4 acc[4][4];
  gemm_core<2048, 1>(smem, lbase, wo, og, 4 * ((mt + nl) & 3), w, l, acc, [](int) {}, [](int, u32x4 (&)[4]) {}, [](int, const u32x4 (&)[4]) {}, false,
                     cnt + ((br * 4 + mt) * 4 + kq) * 32);
  STAMP(7);
  const __amdgpu_buffer_rsrc_t rs = __builtin_amdgcn_make_buffer_rsrc(ows + PART_OFF + (size_t)(br * 32 + nt * 4 + mt) * (16 * 8192), 0, 16 * 8192, 0x00020000);
  if (w < 4) {
    if (w != kq) {
      const int off = (w * 4 + kq) * 8192 + l * 16;
#pragma unroll
      for (int t = 0; t < 4; ++t)
#pragma unroll
        for (int h = 0; h < 2; ++h) {
          u32x4 pk = {pk2(acc[t][2 * h][0], acc[t][2 * h][1]), pk2(acc[t][2 * h][2], acc[t][2 * h][3]),
                      pk2(acc[t][2 * h + 1][0], acc[t][2 * h + 1][1]), pk2(acc[t][2 * h + 1][2], acc[t][2 * h + 1][3])};
#pragma unroll
          for (int i = 0; i < 4; ++i) pk[i] = min(pk[i], 0xFFFFFFFEu);
          __builtin_amdgcn_raw_buffer_store_b128(pk, rs, off + (t * 2 + h) * 1024, 0, SC1);
        }
    } else {
#pragma unroll
      for (int t = 0; t < 4; ++t)
#pragma unroll
        for (int v = 0; v < 4; ++v) *(f32x4*)(smem + (t * 4 + v) * 1024 + l * 16) = acc[t][v];
      WAIT_L0();
    }
  }
  BAR();
  u32x4 pr[3];
  for (unsigned spins = 0; spins < (1u << 18); ++spins) {
    bool ok = true;
#pragma unroll
    for (int s = 0; s < 3; ++s) {
      pr[s] = __builtin_amdgcn_raw_buffer_load_b128(rs, (kq * 4 + s + (s >= kq)) * 8192 + w * 1024 + l * 16, 0, SC1);
#pragma unroll
      for (int i = 0; i < 4; ++i) ok = ok && pr[s][i] != ~0u;
    }
    if (__all(ok)) break;
    asm volatile("" ::: "memory");
  }
#pragma unroll
  for (int i = 0; i < 2; ++i) {
    f32x4 r = *(const f32x4*)(smem + (2 * w + i) * 1024 + l * 16);
#pragma unroll
    for (int s = 0; s < 3; ++s) {
      r[0] += __uint_as_float(pr[s][2 * i] << 16); r[1] += __uint_as_float(pr[s][2 * i] & 0xFFFF0000u);
      r[2] += __uint_as_float(pr[s][2 * i + 1] << 16); r[3] += __uint_as_float(pr[s][2 * i + 1] & 0xFFFF0000u);
    }
    const int mm = mt * 128 + (kq >> 1) * 64 + ((w & 1) * 2 + i) * 16 + c, nn = nt * 128 + (kq & 1) * 64 + (w >> 1) * 16 + g * 4;
    *(u32x2*)(out + ((size_t)mm * 2048 + br * 1024 + nn) * 2) = u32x2{pk2(r[0], r[1]), pk2(r[2], r[3])};
  }
  STAMP(8);
  STAMP_FLUSH(ows);
}

extern "C" void kernel_launch(void* const* d_in, const int* in_sizes, int n_in, void* d_out, int out_size, void* d_ws, size_t ws_size,
                              hipStream_t stream) {
  fused_blocks<<<256, 512, 0, stream>>>((const char*)d_in[0], (const char*)d_in[1], (const char*)d_in[2], (const char*)d_in[3], (const char*)d_in[5],
                                        (const char*)d_in[6], (const char*)d_in[7], (const char*)d_in[8], (char*)d_ws, (const char*)d_in[4],
                                        (const char*)d_in[9], (char*)d_out);
}
```

```cpp
#include <hip/hip_runtime.h>
#include <hip/hip_bf16.h>

typedef short bf16x8 __attribute__((ext_vector_type(8)));
typedef short s16x4 __attribute__((ext_vector_type(4)));
typedef float f32x4 __attribute__((ext_vector_type(4)));
typedef float f32x2 __attribute__((ext_vector_type(2)));
typedef __bf16 bf16x2 __attribute__((ext_vector_type(2)));
typedef unsigned u32x4 __attribute__((ext_vector_type(4)));
typedef unsigned u32x2 __attribute__((ext_vector_type(2)));
#define LAS __attribute__((address_space(3)))
#define DI __device__ __forceinline__
#define RLX_AGENT __ATOMIC_RELAXED, __HIP_MEMORY_SCOPE_AGENT
#define SC1 16

#define WAIT_V(n) asm volatile("s_waitcnt vmcnt(%0)" ::"n"(n) : "memory")
#define WAIT_L0() do { asm volatile("" ::: "memory"); __builtin_amdgcn_s_waitcnt(0xC07F); asm volatile("" ::: "memory"); } while (0)
#define BAR() __builtin_amdgcn_s_barrier()
#define SB() __builtin_amdgcn_sched_barrier(0)

template <bool NT>
DI void glds4(const char* sbase, unsigned v0, unsigned v1, unsigned v2, unsigned v3, unsigned ldsaddr) {
#define G4_(sfx) "s_mov_b32 m0, %5\n\ts_nop 0\n\tglobal_load_lds_dwordx4 %0, %4" sfx "\n\tglobal_load_lds_dwordx4 %1, %4 offset:1024" sfx \
                 "\n\tglobal_load_lds_dwordx4 %2, %4 offset:2048" sfx "\n\tglobal_load_lds_dwordx4 %3, %4 offset:3072" sfx
  if (NT) asm volatile(G4_(" nt")::"v"(v0), "v"(v1), "v"(v2), "v"(v3), "s"(sbase), "s"(ldsaddr) : "memory");
  else asm volatile(G4_("")::"v"(v0), "v"(v1), "v"(v2), "v"(v3), "s"(sbase), "s"(ldsaddr) : "memory");
}
template <bool COH>
DI void glds8(const char* b0, const char* b1, const unsigned (&v)[8], unsigned ldsaddr) {
#define G8_(j, b, off, sfx) "global_load_lds_dwordx4 %" #j ", %" #b " offset:" #off sfx "\n\t"
#define G8A_(sfx) "s_mov_b32 m0, %10\n\ts_nop 0\n\t" G8_(0, 8, -4096, sfx) G8_(1, 8, -3072, sfx) G8_(2, 8, -2048, sfx) G8_(3, 8, -1024, sfx) \
                  G8_(4, 9, 0, sfx) G8_(5, 9, 1024, sfx) G8_(6, 9, 2048, sfx) G8_(7, 9, 3072, sfx)
  if (COH) asm volatile(G8A_(" sc1")::"v"(v[0]), "v"(v[1]), "v"(v[2]), "v"(v[3]), "v"(v[4]), "v"(v[5]), "v"(v[6]), "v"(v[7]), "s"(b0), "s"(b1), "s"(ldsaddr) : "memory");
  else asm volatile(G8A_("")::"v"(v[0]), "v"(v[1]), "v"(v[2]), "v"(v[3]), "v"(v[4]), "v"(v[5]), "v"(v[6]), "v"(v[7]), "s"(b0), "s"(b1), "s"(ldsaddr) : "memory");
}
DI void tile_ld(u32x4 (&r)[4], const char* sbase, unsigned v0, unsigned v1, unsigned v2, unsigned v3) {
  asm volatile("global_load_dwordx4 %0, %4, %8\n\tglobal_load_dwordx4 %1, %5, %8 offset:1024\n\t"
               "global_load_dwordx4 %2, %6, %8 offset:2048\n\tglobal_load_dwordx4 %3, %7, %8 offset:3072"
               : "=&v"(r[0]), "=&v"(r[1]), "=&v"(r[2]), "=&v"(r[3]) : "v"(v0), "v"(v1), "v"(v2), "v"(v3), "s"(sbase) : "memory");
}
DI void tile_st(unsigned ldsaddr, const u32x4 (&r)[4]) {
  asm volatile("ds_write_b128 %0, %1\n\tds_write_b128 %0, %2 offset:1024\n\tds_write_b128 %0, %3 offset:2048\n\tds_write_b128 %0, %4 offset:3072"
               ::"v"(ldsaddr), "v"(r[0]), "v"(r[1]), "v"(r[2]), "v"(r[3]) : "memory");
}

DI f32x4 mfma16(bf16x8 a, bf16x8 b, f32x4 c) { return __builtin_amdgcn_mfma_f32_16x16x32_bf16(a, b, c, 0, 0, 0); }
DI unsigned pk2(float lo, float hi) { f32x2 a = {lo, hi}; return __builtin_bit_cast(unsigned, __builtin_convertvector(a, bf16x2)); }
DI bf16x8 lds_b128(const char* smem, unsigned off) { return *(const bf16x8*)(smem + off); }
DI s16x4 lds_tr(char* smem, unsigned off) { return __builtin_amdgcn_ds_read_tr16_b64_v4i16((LAS s16x4*)(smem + off)); }
DI bf16x8 cat8(s16x4 lo, s16x4 hi) { return __builtin_shufflevector(lo, hi, 0, 1, 2, 3, 4, 5, 6, 7); }
DI float xg_max(float x) {
  auto s = __builtin_amdgcn_permlane16_swap(__float_as_uint(x), __float_as_uint(x), false, false);
  x = fmaxf(__uint_as_float(s[0]), __uint_as_float(s[1]));
  auto t = __builtin_amdgcn_permlane32_swap(__float_as_uint(x), __float_as_uint(x), false, false);
  return fmaxf(__uint_as_float(t[0]), __uint_as_float(t[1]));
}
DI float xg_sum(float x) {
  auto s = __builtin_amdgcn_permlane16_swap(__float_as_uint(x), __float_as_uint(x), false, false);
  x = __uint_as_float(s[0]) + __uint_as_float(s[1]);
  auto t = __builtin_amdgcn_permlane32_swap(__float_as_uint(x), __float_as_uint(x), false, false);
  return __uint_as_float(t[0]) + __uint_as_float(t[1]);
}
DI u32x4 pair16(const f32x4& r0, const f32x4& r1) {
  auto x = __builtin_amdgcn_permlane16_swap(pk2(r0[0], r0[1]), pk2(r1[0], r1[1]), false, false);
  auto y = __builtin_amdgcn_permlane16_swap(pk2(r0[2], r0[3]), pk2(r1[2], r1[3]), false, false);
  return u32x4{x[0], y[0], x[1], y[1]};
}
DI void poll_ge(unsigned* p, unsigned want) {
  unsigned spins = 0;
  while (__hip_atomic_load(p, RLX_AGENT) < want && ++spins < (1u << 18)) __builtin_amdgcn_s_sleep(1);
  asm volatile("" ::: "memory");
}

constexpr float SL2E = 0.088388347648318447f * 1.4426950408889634f;
constexpr size_t CNT_OFF = 8u << 20, Q_OFF = 10u << 20, PART_OFF = 16u << 20;

struct HFrags { s16x4 a[4][2]; bf16x8 b[4]; };
struct FFrags { HFrags h[2]; };
template <int WS, int XT, class TailF, class LandF, class DumpF>
DI void gemm_core(char* smem, unsigned lbase, const char* wg, const char* xg, int rot, int w, int l, f32x4 (&acc)[4][4], TailF tail, LandF land,
                  DumpF dump, bool stream, unsigned* gate) {
  constexpr bool QP = !XT;
  const int c = l & 15, g = l >> 4, q4 = c >> 2, p = l & 3;
  if (w >= 4) {
    u32x4 lr[10][4];
    if (QP && !stream) {
#pragma unroll
      for (int k = 0; k < 10; ++k) land(k, lr[k]);
      WAIT_V(0);
    }
    const int iw = w & 1;
    unsigned vw[8], vx[8];
    {
      const unsigned vw0 = (unsigned)(g * WS + ((c ^ (2 * g)) * 16)), vw1 = vw0 ^ 128;
      const unsigned vx0 = XT ? (unsigned)(l * 16) : (unsigned)((l >> 3) * 2048 + (((l & 7) ^ g) * 16));
      const unsigned vx1 = vx0 ^ 64;
#pragma unroll
      for (int j = 0; j < 8; ++j) {
        vw[j] = ((j & 2) ? vw1 : vw0) + j * (4 * WS - 1024);
        vx[j] = XT ? vx0 + (j >> 1) * (128 * 1024 - 2048) : ((j & 1) ? vx1 : vx0) + j * (16384 - 1024);
      }
    }
    const char* w_w = wg + (size_t)(32 * iw) * WS + 4096;
    const char* x_w = (XT ? xg + (size_t)(4 * iw) * (128 * 1024) : xg + (size_t)(64 * iw) * 2048) + 4096;
    const unsigned lw0 = lbase + iw * 8192 + 4096;
    auto wpart = [&](int kap, unsigned buf) { const char* s = w_w + (size_t)kap * (64 * WS); glds8<false>(s, s, vw, lw0 + buf); };
    auto xpart = [&](int kap, unsigned buf) { const char* s = x_w + kap * (XT ? 2048 : 128); glds8<XT != 0>(s, s, vx, lw0 + buf + 16384); };
    auto stage = [&](int kap, unsigned buf) { wpart(kap & 15, buf); xpart(kap & 15, buf); };
    if (w < 6) {
      if (XT) {
#pragma unroll 1
        for (int f = 0; f < 2; ++f) wpart(rot + f, f * 32768);
        if (w == 4) poll_ge(gate, 8u);
        BAR();
#pragma unroll 1
        for (int f = 0; f < 2; ++f) xpart(rot + f, f * 32768);
        WAIT_V(8);
      } else {
        stage(rot, 0);
        WAIT_V(0);
      }
#pragma unroll 1
      for (int i = 0; i < 2; ++i) {
        WAIT_V(16);
        BAR();
        if (QP && i == 0) stage(rot + 1, 32768);
        BAR();
        stage(rot + i + 2, i * 32768);
        BAR();
        BAR();
      }
      WAIT_V(16);
      BAR();
      BAR();
      if (QP && stream) tail(0);
      BAR();
      if (QP && stream) tail(1);
      BAR();
      if (QP && stream) { tail(2); WAIT_V(12); } else WAIT_V(0);
      BAR();
      if (QP && stream) tail(3);
      BAR();
      BAR();
      BAR();
    } else {
      __builtin_amdgcn_s_sleep(12);
      wpart((rot + 4) & 15, 65536);
      if (XT) BAR();
      xpart((rot + 4) & 15, 65536);
      BAR();
      stage(rot + 8, 98304);
      WAIT_V(16);
      BAR();
      stage(rot + 12, 131072);
#pragma unroll 1
      for (int i = 0; i < 3; ++i) {
        WAIT_V(16);
        BAR();
        stage(rot + 5 + i, 65536);
        WAIT_V(16);
        BAR();
        stage(rot + 9 + i, 98304);
        BAR();
        stage(rot + 13 + i, 131072);
        WAIT_V(32);
        BAR();
      }
      if (QP && stream) { tail(0); tail(1); WAIT_V(24); } else WAIT_V(16);
      BAR();
      if (QP && stream) { tail(2); tail(3); WAIT_V(16); } else WAIT_V(0);
      BAR();
    }
    if (QP && !stream) {
#pragma unroll
      for (int k = 0; k < 8; ++k) dump(k, lr[k]);
      BAR();
      dump(8, lr[8]);
      dump(9, lr[9]);
      asm volatile("s_waitcnt lgkmcnt(0)" ::: "memory");
    }
  } else {
    if (XT) BAR();
    const int nh = w & 1, mh = w >> 1;
    unsigned aw[4];
    {
      const int F = 2 * q4 | 8 * (g & 1);
#pragma unroll
      for (int t = 0; t < 4; ++t) aw[t] = (unsigned)((8 * g + q4) * 256 + ((((2 * (4 * nh + t)) ^ F) + (p >> 1)) * 16) + (p & 1) * 8);
    }
    const unsigned ax0 = XT ? (unsigned)(16384 + mh * 8192 + l * 16) : (unsigned)(16384 + (64 * mh + c) * 128 + ((g ^ (c >> 1)) * 16));
    const unsigned ax1 = XT ? ax0 + 1024 : (ax0 ^ 64);
    auto mm = [&](const FFrags& fo) {
#pragma unroll
      for (int ks = 0; ks < 2; ++ks)
#pragma unroll
        for (int t = 0; t < 4; ++t)
#pragma unroll
          for (int v = 0; v < 4; ++v) acc[t][v] = mfma16(cat8(fo.h[ks].a[t][0], fo.h[ks].a[t][1]), fo.h[ks].b[v], acc[t][v]);
    };
    auto step = [&](FFrags& fn, const FFrags& fo, unsigned bo) {
#pragma unroll
      for (int ks = 0; ks < 2; ++ks)
#pragma unroll
        for (int t = 0; t < 4; ++t) {
          fn.h[ks].b[t] = lds_b128(smem, (ks ? ax1 : ax0) + bo + t * 2048);
          fn.h[ks].a[t][0] = lds_tr(smem, aw[t] + bo + ks * 8192);
          fn.h[ks].a[t][1] = lds_tr(smem, aw[t] + bo + ks * 8192 + 1024);
        }
      mm(fo);
#pragma unroll
      for (int t = 0; t < 24; ++t) {
        __builtin_amdgcn_sched_group_barrier(0x008, 1, 0);
        __builtin_amdgcn_sched_group_barrier(0x100, 1, 0);
      }
      __builtin_amdgcn_sched_group_barrier(0x008, 8, 0);
      SB();
    };
#pragma unroll
    for (int t = 0; t < 4; ++t)
#pragma unroll
      for (int v = 0; v < 4; ++v) acc[t][v] = f32x4{0, 0, 0, 0};
    FFrags f0, f1;
#pragma unroll
    for (int ks = 0; ks < 2; ++ks)
#pragma unroll
      for (int t = 0; t < 4; ++t) { f1.h[ks].b[t] = bf16x8{0, 0, 0, 0, 0, 0, 0, 0}; f1.h[ks].a[t][0] = s16x4{0, 0, 0, 0}; f1.h[ks].a[t][1] = s16x4{0, 0, 0, 0}; }
#pragma unroll 1
    for (int n = 0; n < 16; n += 2) {
      const unsigned bo0 = (n & 2) ? 98304 : ((n >> 2) & 1) * 32768, bo1 = (n & 2) ? 131072 : 65536;
      WAIT_L0();
      BAR();
      step(f0, f1, bo0);
      WAIT_L0();
      BAR();
      step(f1, f0, bo1);
    }
    WAIT_L0();
    if (QP && !stream) BAR();
    mm(f1);
  }
}

__global__ __launch_bounds__(512, 2) void fused_blocks(const char* __restrict__ xA, const char* __restrict__ kA, const char* __restrict__ vA,
                                                       const char* __restrict__ wqA, const char* __restrict__ xB, const char* __restrict__ kB,
                                                       const char* __restrict__ vB, const char* __restrict__ wqB, char* __restrict__ ows,
                                                       const char* __restrict__ woA, const char* __restrict__ woB, char* __restrict__ out) {
  __shared__ __attribute__((aligned(1024))) char smem[163840];
  asm volatile("" ::"s"(xA), "s"(kA), "s"(vA), "s"(wqA), "s"(xB), "s"(kB), "s"(vB), "s"(wqB), "s"(ows), "s"(woA), "s"(woB), "s"(out));
  const unsigned lbase = (unsigned)(size_t)(LAS char*)smem;
  const int tid = threadIdx.x, l = tid & 63, w = __builtin_amdgcn_readfirstlane(tid >> 6);
  const int xcd = blockIdx.x & 7, j = blockIdx.x >> 3;
  const int br = j & 1, idx = j >> 1;
  const int bg = idx & 3, qhead = 4 * xcd + (idx >> 2), rot = 4 * (((idx >> 2) + bg) & 3);
  const char* xg = (br ? xB : xA) + (size_t)bg * 128 * 2048;
  const char* wq = (br ? wqB : wqA) + qhead * 256;
  const int c = l & 15, g = l >> 4, q4 = c >> 2, p = l & 3;
  const int u = w & 3, dh = w >> 2;
  const int b = br ? 4 * bg + u : (xcd & 1) * 8 + (idx >> 1);
  const int head = br ? qhead : 8 * (xcd >> 1) + 4 * (idx & 1) + u;
  auto ring_of = [](int ww) { return (unsigned)((ww >= 4 ? ww - 4 : ww < 2 ? ww + 4 : ww + 6) * 16384); };
  const unsigned ring = ring_of(w);
  if (w == 2 && l < 12) {
    const int uu = l & 3;
    const size_t kvo = ((size_t)(br ? (4 * bg + uu) * 32 + qhead : b * 4 + (xcd >> 1)) * 512) * 256;
    const char* a = l < 4 ? wq + (size_t)uu * (2u << 20) : l < 8 ? (br ? kB : kA) + kvo : (br ? vB : vA) + kvo;
    const unsigned t_ = __builtin_nontemporal_load((const unsigned*)a);
    asm volatile("" ::"v"(t_));
  }
  const size_t kvoff = ((size_t)(br ? b * 32 + head : b * 4 + (xcd >> 1)) * 512 + dh * 256) * 256;
  const char* kg = (br ? kB : kA) + kvoff;
  const char* vg = (br ? vB : vA) + kvoff;
  unsigned vk[4], vv[2];
#pragma unroll
  for (int i = 0; i < 4; ++i) vk[i] = (unsigned)(g * 256 + ((c ^ g ^ (4 * i)) * 16));
#pragma unroll
  for (int i = 0; i < 2; ++i) vv[i] = (unsigned)(g * 256 + ((c ^ (2 * g) ^ (8 * i)) * 16));
  const int r16 = br ? 2 * w + idx : (idx >> 1) * 2 + xcd;
  auto tile_to = [&](int blk, int isV, int t, unsigned base) {
    const unsigned la = lbase + base + t * 4096;
    const char* src = (isV ? vg : kg) + ((blk * 4 + t + r16) & 15) * 4096;
    if (br) {
      if (isV) glds4<true>(src, vv[0], vv[1], vv[0], vv[1], la);
      else glds4<true>(src, vk[0], vk[1], vk[2], vk[3], la);
    } else {
      if (isV) glds4<false>(src, vv[0], vv[1], vv[0], vv[1], la);
      else glds4<false>(src, vk[0], vk[1], vk[2], vk[3], la);
    }
  };
  auto tile_issue = [&](int blk, int isV, int t) { tile_to(blk, isV, t, ring); };

  unsigned* ctl = (unsigned*)(ows + CNT_OFF);
  const __amdgpu_buffer_rsrc_t rc = __builtin_amdgcn_make_buffer_rsrc(ows + CNT_OFF, 0, 32768, 0x00020000);
  unsigned nonce = 0;
  auto echo = [&]() {
    const u32x4 r = __builtin_amdgcn_raw_buffer_load_b128(rc, l * 16, 0, SC1);
    __builtin_amdgcn_raw_buffer_store_b128(r, rc, 4096 + l * 16, 0, SC1);
  };
  if (w == 0) {
    nonce = __hip_atomic_load(ctl + 1024 + blockIdx.x, RLX_AGENT) + 1u;
    if (l == 0) __hip_atomic_store(ctl + blockIdx.x, nonce, RLX_AGENT);
  } else if (w == 1 && blockIdx.x == 0) {
#pragma unroll
    for (int i = 0; i < 2; ++i) __builtin_amdgcn_raw_buffer_store_b32(0u, rc, 8192 + (i * 64 + l) * 128, 0, SC1);
    WAIT_V(0);
    __builtin_amdgcn_s_sleep(24);
    echo();
  }
  unsigned* cnt = ctl + 2048 + 32;
  auto confirm = [&]() {
    unsigned spins = 0;
    while (__hip_atomic_load(ctl + 1024 + blockIdx.x, RLX_AGENT) != nonce && ++spins < (1u << 18)) __builtin_amdgcn_s_sleep(2);
    if (l == 0) __hip_atomic_fetch_add(ctl + 2048, 1u, RLX_AGENT);
  };
  const int kq = xcd >> 1, mt = idx & 3, nl = idx >> 2, nt = (xcd & 1) * 4 + nl;
  if (!br && w < 4) {
    unsigned vp[8];
#pragma unroll
    for (int i = 0; i < 4; ++i) {
      vp[i] = (unsigned)(g * 8192 + c * 16 + i * (4 * 8192 - 1024));
      vp[4 + i] = (unsigned)((l >> 3) * 2048 + (l & 7) * 16 + i * (16384 - 1024));
    }
    const char* wb = wqB + qhead * 256 + (size_t)(16 * w) * 8192 + 4096;
    const char* xb = xB + (size_t)(bg * 128 + 32 * w) * 2048;
#pragma unroll 1
    for (int k = 1; k < 4; ++k) glds8<false>(wb + (size_t)(rot + k) * (64 * 8192), xb + (rot + k) * 128, vp, lbase + 131072 + w * 8192 + 4096);
    WAIT_V(0);
    if (w >= 1) {
      const int src = (w - 1) + ((w - 1) >= kq);
      const __amdgpu_buffer_rsrc_t r2 = __builtin_amdgcn_make_buffer_rsrc(ows + PART_OFF + (size_t)(nt * 4 + mt) * (16 * 8192), 0, 33 * 16 * 8192, 0x00020000);
#pragma unroll
      for (int i = 0; i < 16; ++i)
        __builtin_amdgcn_raw_buffer_store_b128(u32x4{~0u, ~0u, ~0u, ~0u}, r2, (i >> 3) * (32 * 16 * 8192) + (kq * 4 + src) * 8192 + (i & 7) * 1024 + l * 16, 0, SC1);
      if (w == 1 && blockIdx.x == 0) echo();
      WAIT_V(0);
    }
  }

  f32x4 qa[4][4];
  auto land = [&](int k, u32x4 (&r)[4]) {
    const int ph = k >> 1, hf = k & 1;
    const char* src = ((ph & 1) ? vg : kg) + (hf - 1) * 65536 + (((ph >> 1) * 4 + u + r16) & 15) * 4096;
    if (ph & 1) tile_ld(r, src, vv[0], vv[1], vv[0], vv[1]);
    else tile_ld(r, src, vk[0], vk[1], vk[2], vk[3]);
  };
  auto dump = [&](int k, const u32x4 (&r)[4]) { tile_st(lbase + k * 16384 + u * 4096 + l * 16, r); };
  gemm_core<8192, 0>(smem, lbase, wq, xg, rot, w, l, qa, [&](int n) { tile_issue(0, 0, n); }, land, dump, br != 0, nullptr);
  bf16x8 qf[2][4];
  if (br) {
    if (w < 4) {
#pragma unroll
      for (int t = 0; t < 4; ++t)
#pragma unroll
        for (int v = 0; v < 4; ++v) {
          const unsigned off = 98304 + ((w >> 1) * 4 + v) * 4096 + (w & 1) * 2048 + t * 512 + ((g >> 1) * 16 + c) * 16 + (g & 1) * 8;
          *(u32x2*)(smem + off) = u32x2{pk2(qa[t][v][0], qa[t][v][1]), pk2(qa[t][v][2], qa[t][v][3])};
        }
    }
    WAIT_L0();
    BAR();
#pragma unroll
    for (int v = 0; v < 2; ++v)
#pragma unroll
      for (int s = 0; s < 4; ++s) qf[v][s] = lds_b128(smem, 98304 + u * 8192 + v * 4096 + s * 1024 + l * 16);
    if (w < 4) {
#pragma unroll 1
      for (int t = 0; t < 4; ++t) tile_issue(0, 0, t);
    }
    if (w == 0) confirm();
  } else {
    const __amdgpu_buffer_rsrc_t rq = __builtin_amdgcn_make_buffer_rsrc(ows + Q_OFF, 0, 4u << 20, 0x00020000);
    if (w < 4) {
      const int qo = ((bg * 8 + (w >> 1) * 4) * 128 + qhead * 4 + (w & 1) * 2) * 1024 + ((g >> 1) * 16 + c) * 16 + (g & 1) * 512;
#pragma unroll
      for (int t = 0; t < 4; t += 2)
#pragma unroll
        for (int v = 0; v < 4; ++v) __builtin_amdgcn_raw_buffer_store_b128(pair16(qa[t][v], qa[t + 1][v]), rq, qo + v * (128 * 1024) + t * 512, 0, SC1);
      if (w == 1 && blockIdx.x == 0) echo();
      if (w == 0) confirm();
      WAIT_V(0);
    }
    BAR();
    if (tid == 0) __hip_atomic_fetch_add(cnt + (32 + xcd * 4 + bg) * 32, 1u, RLX_AGENT);
    if (w == 1 && blockIdx.x == 0) {
      unsigned spins = 0;
      while (__hip_atomic_load(ctl + 2048, RLX_AGENT) < 256u && ++spins < (1u << 17)) { echo(); __builtin_amdgcn_s_sleep(8); }
    }
    if (w == 4) poll_ge(cnt + (32 + (head >> 2) * 4 + (b >> 2)) * 32, 4u);
    BAR();
#pragma unroll
    for (int v = 0; v < 2; ++v)
#pragma unroll
      for (int s = 0; s < 4; ++s)
        qf[v][s] = __builtin_bit_cast(bf16x8, __builtin_amdgcn_raw_buffer_load_b128(rq, ((2 * b + v) * 128 + head * 4 + s) * 1024 + l * 16, 0, SC1));
    WAIT_V(0);
  }

  unsigned ak[4], av[8];
#pragma unroll
  for (int s = 0; s < 4; ++s) ak[s] = (unsigned)(c * 256 + ((g ^ c ^ (4 * s)) * 16));
  {
    const int row = 4 * g + q4, F = 2 * (row & 7);
#pragma unroll
    for (int t = 0; t < 8; ++t) av[t] = (unsigned)(row * 256 + ((((2 * t) ^ F) + (p >> 1)) * 16) + (p & 1) * 8);
  }
  f32x4 o[8][2];
#pragma unroll
  for (int t = 0; t < 8; ++t) { o[t][0] = f32x4{0, 0, 0, 0}; o[t][1] = f32x4{0, 0, 0, 0}; }
  unsigned tb = ring, pnext = dh * 16384u;
  float m[2] = {-__builtin_inff(), -__builtin_inff()}, ls[2] = {0.f, 0.f};
#pragma unroll 1
  for (int blk = 0; blk < 4; ++blk) {
    f32x4 sc[4][2];
    if (br) {
      if (blk == 0) __builtin_amdgcn_s_setprio(3); else if (blk == 2) __builtin_amdgcn_s_setprio(2); else if (blk == 3) __builtin_amdgcn_s_setprio(0);
    } else {
      if (blk == 3) WAIT_V(4);
      BAR();
      if (blk == 1) tile_to(3, 0, u, tb);
      tb = pnext; pnext = pnext >= 4 * 32768u ? pnext - 4 * 32768u : pnext + 32768u;
    }
#pragma unroll
    for (int t = 0; t < 4; ++t) {
      if (br) WAIT_V(12);
      bf16x8 kf[4];
#pragma unroll
      for (int s = 0; s < 4; ++s) kf[s] = lds_b128(smem, ak[s] + tb + t * 4096);
      WAIT_L0();
      if (br) tile_issue(blk, 1, t);
      sc[t][0] = f32x4{0, 0, 0, 0}; sc[t][1] = f32x4{0, 0, 0, 0};
#pragma unroll
      for (int s = 0; s < 4; ++s)
#pragma unroll
        for (int v = 0; v < 2; ++v) sc[t][v] = mfma16(kf[s], qf[v][s], sc[t][v]);
      SB();
    }
    bf16x8 pf[2][2];
#pragma unroll
    for (int v = 0; v < 2; ++v) {
      float mx = sc[0][v][0];
#pragma unroll
      for (int t = 0; t < 4; ++t)
#pragma unroll
        for (int i = 0; i < 4; ++i) mx = fmaxf(mx, sc[t][v][i]);
      mx = xg_max(mx) * SL2E;
      const float mn = fmaxf(m[v], mx);
      const float alpha = __builtin_amdgcn_exp2f(m[v] - mn);
      ls[v] *= alpha;
#pragma unroll
      for (int t = 0; t < 8; ++t) o[t][v] *= alpha;
      m[v] = mn;
      float sum = 0.f;
#pragma unroll
      for (int t = 0; t < 4; ++t)
#pragma unroll
        for (int i = 0; i < 4; ++i) { const float e = __builtin_amdgcn_exp2f(fmaf(sc[t][v][i], SL2E, -mn)); sc[t][v][i] = e; sum += e; }
      ls[v] += sum;
#pragma unroll
      for (int kb = 0; kb < 2; ++kb) {
        u32x4 pw = {pk2(sc[2 * kb][v][0], sc[2 * kb][v][1]), pk2(sc[2 * kb][v][2], sc[2 * kb][v][3]),
                    pk2(sc[2 * kb + 1][v][0], sc[2 * kb + 1][v][1]), pk2(sc[2 * kb + 1][v][2], sc[2 * kb + 1][v][3])};
        pf[kb][v] = __builtin_bit_cast(bf16x8, pw);
      }
    }
    const bool more = blk < 3;
    if (br) {
      if (blk == 2) __builtin_amdgcn_s_setprio(1);
    } else {
      if (blk == 2) WAIT_V(8); else if (blk == 3) WAIT_V(0);
      BAR();
      if (blk < 2) tile_to(blk + 2, 1, u, tb);
      tb = pnext; pnext = pnext >= 4 * 32768u ? pnext - 4 * 32768u : pnext + 32768u;
    }
#pragma unroll
    for (int kb = 0; kb < 2; ++kb) {
      s16x4 vlo[8], vhi[8];
      if (br) { if (kb == 0 || more) WAIT_V(12); else WAIT_V(4); }
#pragma unroll
      for (int t = 0; t < 8; ++t) vlo[t] = lds_tr(smem, av[t] + tb + (2 * kb) * 4096);
      if (br) {
        WAIT_L0();
        if (more) { tile_issue(blk + 1, 0, 2 * kb); WAIT_V(12); }
        else if (kb == 0) WAIT_V(8);
        else WAIT_V(0);
      }
#pragma unroll
      for (int t = 0; t < 8; ++t) vhi[t] = lds_tr(smem, av[t] + tb + (2 * kb + 1) * 4096);
      WAIT_L0();
      if (br && more) tile_issue(blk + 1, 0, 2 * kb + 1);
#pragma unroll
      for (int t = 0; t < 8; ++t)
#pragma unroll
        for (int v = 0; v < 2; ++v) o[t][v] = mfma16(cat8(vlo[t], vhi[t]), pf[kb][v], o[t][v]);
      SB();
    }
  }
  if (!br) BAR();
  ls[0] = xg_sum(ls[0]);
  ls[1] = xg_sum(ls[1]);
  if (dh) {
#pragma unroll
    for (int t = 0; t < 8; ++t) { const f32x4 tmp = o[t][0]; o[t][0] = o[t][1]; o[t][1] = tmp; }
    float tm = m[0]; m[0] = m[1]; m[1] = tm;
    tm = ls[0]; ls[0] = ls[1]; ls[1] = tm;
  }
#pragma unroll
  for (int t = 0; t < 8; ++t) *(f32x4*)(smem + ring + t * 1024 + l * 16) = o[t][1];
  *(f32x2*)(smem + ring + 8192 + l * 8) = f32x2{m[1], ls[1]};
  WAIT_L0();
  BAR();
  {
    const unsigned pring = ring_of(w ^ 4);
    const f32x2 ml = *(const f32x2*)(smem + pring + 8192 + l * 8);
    const float mm = fmaxf(m[0], ml[0]);
    float a0 = __builtin_amdgcn_exp2f(m[0] - mm), a1 = __builtin_amdgcn_exp2f(ml[0] - mm);
    const float inv = 1.0f / (ls[0] * a0 + ml[1] * a1);
    a0 *= inv; a1 *= inv;
    const __amdgpu_buffer_rsrc_t ro = __builtin_amdgcn_make_buffer_rsrc(ows + (size_t)br * (4u << 20) + ((size_t)(2 * b + dh) * 128 + head * 4) * 1024, 0, 4096, 0x00020000);
#pragma unroll
    for (int t = 0; t < 8; t += 2) {
      const f32x4 r0 = o[t][0] * a0 + *(const f32x4*)(smem + pring + t * 1024 + l * 16) * a1;
      const f32x4 r1 = o[t + 1][0] * a0 + *(const f32x4*)(smem + pring + (t + 1) * 1024 + l * 16) * a1;
      __builtin_amdgcn_raw_buffer_store_b128(pair16(r0, r1), ro, (t + (g & 1)) * 512 + ((g >> 1) * 16 + c) * 16, 0, SC1);
    }
  }
  WAIT_V(0);
  WAIT_L0();
  BAR();
  if (tid == 0) __hip_atomic_fetch_add(cnt + ((br * 4 + (b >> 2)) * 4 + (head >> 3)) * 32, 1u, RLX_AGENT);

  if (w == 5) poll_ge(ctl + 2048, 256u);
  const char* og = ows + (size_t)br * (4u << 20) + ((size_t)(mt * 8) * 128 + kq * 32) * 1024;
  const char* wo = (br ? woB : woA) + (size_t)(kq * 1024) * 2048 + nt * 256;
  f32x4 acc[4][4];
  gemm_core<2048, 1>(smem, lbase, wo, og, 4 * ((mt + nl) & 3), w, l, acc, [](int) {}, [](int, u32x4 (&)[4]) {}, [](int, const u32x4 (&)[4]) {}, false,
                     cnt + ((br * 4 + mt) * 4 + kq) * 32);
  const __amdgpu_buffer_rsrc_t rs = __builtin_amdgcn_make_buffer_rsrc(ows + PART_OFF + (size_t)(br * 32 + nt * 4 + mt) * (16 * 8192), 0, 16 * 8192, 0x00020000);
  if (w < 4) {
    if (w != kq) {
      const int off = (w * 4 + kq) * 8192 + l * 16;
#pragma unroll
      for (int t = 0; t < 4; ++t)
#pragma unroll
        for (int h = 0; h < 2; ++h) {
          u32x4 pk = {pk2(acc[t][2 * h][0], acc[t][2 * h][1]), pk2(acc[t][2 * h][2], acc[t][2 * h][3]),
                      pk2(acc[t][2 * h + 1][0], acc[t][2 * h + 1][1]), pk2(acc[t][2 * h + 1][2], acc[t][2 * h + 1][3])};
#pragma unroll
          for (int i = 0; i < 4; ++i) pk[i] = min(pk[i], 0xFFFFFFFEu);
          __builtin_amdgcn_raw_buffer_store_b128(pk, rs, off + (t * 2 + h) * 1024, 0, SC1);
        }
    } else {
#pragma unroll
      for (int t = 0; t < 4; ++t)
#pragma unroll
        for (int v = 0; v < 4; ++v) *(f32x4*)(smem + (t * 4 + v) * 1024 + l * 16) = acc[t][v];
      WAIT_L0();
    }
  }
  BAR();
  u32x4 pr[3];
  for (unsigned spins = 0; spins < (1u << 18); ++spins) {
    bool ok = true;
#pragma unroll
    for (int s = 0; s < 3; ++s) {
      pr[s] = __builtin_amdgcn_raw_buffer_load_b128(rs, (kq * 4 + s + (s >= kq)) * 8192 + w * 1024 + l * 16, 0, SC1);
#pragma unroll
      for (int i = 0; i < 4; ++i) ok = ok && pr[s][i] != ~0u;
    }
    if (__all(ok)) break;
    asm volatile("" ::: "memory");
  }
#pragma unroll
  for (int i = 0; i < 2; ++i) {
    f32x4 r = *(const f32x4*)(smem + (2 * w + i) * 1024 + l * 16);
#pragma unroll
    for (int s = 0; s < 3; ++s) {
      r[0] += __uint_as_float(pr[s][2 * i] << 16); r[1] += __uint_as_float(pr[s][2 * i] & 0xFFFF0000u);
      r[2] += __uint_as_float(pr[s][2 * i + 1] << 16); r[3] += __uint_as_float(pr[s][2 * i + 1] & 0xFFFF0000u);
    }
    const int mm = mt * 128 + (kq >> 1) * 64 + ((w & 1) * 2 + i) * 16 + c, nn = nt * 128 + (kq & 1) * 64 + (w >> 1) * 16 + g * 4;
    *(u32x2*)(out + ((size_t)mm * 2048 + br * 1024 + nn) * 2) = u32x2{pk2(r[0], r[1]), pk2(r[2], r[3])};
  }
}

extern "C" void kernel_launch(void* const* d_in, const int* in_sizes, int n_in, void* d_out, int out_size, void* d_ws, size_t ws_size,
                              hipStream_t stream) {
  fused_blocks<<<256, 512, 0, stream>>>((const char*)d_in[0], (const char*)d_in[1], (const char*)d_in[2], (const char*)d_in[3], (const char*)d_in[5],
                                        (const char*)d_in[6], (const char*)d_in[7], (const char*)d_in[8], (char*)d_ws, (const char*)d_in[4],
                                        (const char*)d_in[9], (char*)d_out);
}
```
